```python
import torch

def _softmax(x, axis=-1):
    return torch.softmax(x, dim=axis)
def _gelu(x):
    return torch.nn.functional.gelu(x, approximate="tanh")
def _silu(x):
    return torch.nn.functional.silu(x)
def _sigmoid(x):
    return torch.sigmoid(x)
def _layernorm(x, axis=-1, eps=1e-5):
    m = x.mean(dim=axis, keepdim=True)
    v = x.var(dim=axis, keepdim=True, unbiased=False)
    return (x - m) / torch.sqrt(v + eps)
def _scatter_add(tgt, idx, src, axis=0):
    return tgt.clone().index_add_(axis, idx, src)
def _segment_sum(src, seg, n):
    out = torch.zeros((n,) + src.shape[1:], dtype=src.dtype)
    return out.index_add_(0, seg, src)
def _pad(x, pw):
    flat = tuple(v for pair in reversed(pw) for v in pair)
    return torch.nn.functional.pad(x, flat)

kernel_name = 'parallel_sum__ssm_conv_h3__geglu_ffn__prompt'

def setup_inputs():

    _w = (lambda a: (lambda t: t.to(torch.bfloat16) if t.is_floating_point() else t)(torch.as_tensor(a)))
    def _A_setup():
        B, S, D = 1, 2048, 512
        ks = 4
        return {
            "q": _w((lambda *s: torch.randn(*s, dtype=torch.bfloat16) * 0.05)(B, S, D)),
            "k": _w((lambda *s: torch.randn(*s, dtype=torch.bfloat16) * 0.8)(B, S, D)),
            "v": _w((lambda *s: torch.randn(*s, dtype=torch.bfloat16) * 0.8)(B, S, D)),
            "shift_k": _w((lambda *s: torch.randn(*s, dtype=torch.bfloat16) * 0.8)(D, ks) * 0.5),
            "ssm_k": _w((lambda *s: torch.randn(*s, dtype=torch.bfloat16) * 0.8)(D, S) * (1.0 / S ** 0.5)),
            "ssm_D": _w((lambda *s: torch.randn(*s, dtype=torch.bfloat16) * 0.8)(D)),
        }
    _A_in = _A_setup()

    def _B_setup():
        B, S, D, F = 1, 2048, 512, 1792
        return {
            "x": _w((lambda *s: torch.randn(*s, dtype=torch.bfloat16) * 0.05)(B, S, D)),
            "w_gate": _w((lambda *s: torch.randn(*s, dtype=torch.bfloat16) * 0.05)(D, F)),
            "w_up": _w((lambda *s: torch.randn(*s, dtype=torch.bfloat16) * 0.05)(D, F)),
            "w_down": _w((lambda *s: torch.randn(*s, dtype=torch.bfloat16) * 0.05)(F, D)),
        }
    _B_in = _B_setup()

    return {
        "A_q": _A_in["q"],
        "A_k": _A_in["k"],
        "A_v": _A_in["v"],
        "A_shift_k": _A_in["shift_k"],
        "A_ssm_k": _A_in["ssm_k"],
        "A_ssm_D": _A_in["ssm_D"],
        "B_x": _B_in["x"],
        "B_w_gate": _B_in["w_gate"],
        "B_w_up": _B_in["w_up"],
        "B_w_down": _B_in["w_down"],
    }

def reference(A_q, A_k, A_v, A_shift_k, A_ssm_k, A_ssm_D, B_x, B_w_gate, B_w_up, B_w_down):
    def _A_ref(q, k, v, shift_k, ssm_k, ssm_D):
        B, S, D = q.shape
        ks = shift_k.shape[-1]
        k_pad = _pad(k, ((0, 0), (ks - 1, 0), (0, 0)))
        k_shift = torch.zeros_like(k)
        for t in range(ks):
            k_shift = k_shift + k_pad[:, t:t + S, :] * shift_k[:, ks - 1 - t][None, None, :]
        kv = k_shift * v
        L = 2 * S
        kv_pad = _pad(kv, ((0, 0), (0, S), (0, 0)))
        kf_pad = _pad(ssm_k, ((0, 0), (0, S)))
        kv_f = (lambda x, axes=(-2,-1): torch.fft.rfft2(x.float(), dim=axes))(kv_pad[:, None, :, :], axes=(1, 2))
        kf_f = (lambda x, axes=(-2,-1): torch.fft.rfft2(x.float(), dim=axes))(kf_pad[:, None, :], axes=(1, 2))
        kf_f = (lambda x, *d: x.permute(*d))(kf_f, 1, 2, 0)
        y_f = kv_f * kf_f[None, :, :, :]
        y = (lambda x, s, axes=(-2,-1): torch.fft.irfft2(x, s=s, dim=axes).to(torch.bfloat16))(y_f, (1, L), axes=(1, 2))[:, 0, :S, :]
        y = y + kv * ssm_D[None, None, :]
        return q * y

    def _B_ref(x, w_gate, w_up, w_down):
        return (_gelu(x @ w_gate) * (x @ w_up)) @ w_down

    x = A_q
    xn = _layernorm(x, axis=-1)
    return x + _A_ref(xn, A_k, A_v, A_shift_k, A_ssm_k, A_ssm_D) + _B_ref(xn, B_w_gate, B_w_up, B_w_down)
```

```cpp
#include <hip/hip_runtime.h>
#include <hip/hip_bf16.h>
#include <stdint.h>
typedef unsigned short u16;
constexpr int S=2048, D=512, F=1792;
__device__ __forceinline__ float b2f(u16 b){ return __uint_as_float(((unsigned)b)<<16); }
__device__ __forceinline__ u16 f2b(float f){ unsigned u=__float_as_uint(f); u += 0x7FFFu + ((u>>16)&1u); return (u16)(u>>16); }
__device__ __forceinline__ float rb(float f){ return b2f(f2b(f)); }

__global__ void k_ln(const u16* x, const u16* k, const u16* v, const u16* sk, u16* xn, u16* kv){
  int row=blockIdx.x; int l=threadIdx.x;
  float xv[8]; float s=0;
  for(int j=0;j<8;j++){ xv[j]=b2f(x[row*D+l*8+j]); s+=xv[j]; }
  for(int o=32;o>0;o>>=1) s+=__shfl_xor(s,o);
  float mf=s/D; float m=rb(mf);
  float q=0; for(int j=0;j<8;j++){ float d=xv[j]-mf; q+=d*d; }
  for(int o=32;o>0;o>>=1) q+=__shfl_xor(q,o);
  float var=rb(q/D);
  float den=rb(sqrtf(rb(var+1e-5f)));
  for(int j=0;j<8;j++){ int d=l*8+j; xn[row*D+d]=f2b(rb(xv[j]-m)/den);
    float acc=0;
    for(int t=0;t<4;t++){ int r=row-3+t; float kk = r>=0? b2f(k[r*D+d]):0.f; acc=rb(acc+rb(kk*b2f(sk[d*4+3-t]))); }
    kv[row*D+d]=f2b(acc*b2f(v[row*D+d]));
  }
}
__global__ void k_conv(const u16* kv, const u16* h, float* y){
  int d=blockIdx.x*64+ (threadIdx.x&63); int t=blockIdx.y*4+(threadIdx.x>>6);
  float acc=0; for(int s=0;s<=t;s++) acc+=b2f(h[d*S+t-s])*b2f(kv[s*D+d]);
  y[t*D+d]=acc;
}
__global__ void k_g1(const u16* xn, const u16* wg, const u16* wu, u16* H){
  int f=blockIdx.x*64+(threadIdx.x&63); int t=blockIdx.y*4+(threadIdx.x>>6);
  float g=0,u=0; for(int k=0;k<D;k++){ float a=b2f(xn[t*D+k]); g+=a*b2f(wg[k*F+f]); u+=a*b2f(wu[k*F+f]); }
  g=rb(g); u=rb(u);
  float ge=0.5f*g*(1.f+tanhf(0.7978845608028654f*(g+0.044715f*g*g*g)));
  H[t*F+f]=f2b(rb(ge)*u);
}
__global__ void k_g2(const u16* H, const u16* wd, const u16* x, const u16* xn, const u16* kv, const float* y, const u16* Dv, u16* out){
  int d=blockIdx.x*64+(threadIdx.x&63); int t=blockIdx.y*4+(threadIdx.x>>6);
  float o=0; for(int k=0;k<F;k++) o+=b2f(H[t*F+k])*b2f(wd[k*D+d]);
  o=rb(o);
  float yy=rb(rb(y[t*D+d])+rb(b2f(kv[t*D+d])*b2f(Dv[d])));
  float a=rb(b2f(xn[t*D+d])*yy);
  out[t*D+d]=f2b(rb(b2f(x[t*D+d])+a)+o);
}
extern "C" void kernel_launch(void* const* d_in, const int* in_sizes, int n_in, void* d_out, int out_size, void* d_ws, size_t ws_size, hipStream_t stream){
  const u16* q=(const u16*)d_in[0]; const u16* k=(const u16*)d_in[1]; const u16* v=(const u16*)d_in[2];
  const u16* sk=(const u16*)d_in[3]; const u16* h=(const u16*)d_in[4]; const u16* Dv=(const u16*)d_in[5];
  const u16* wg=(const u16*)d_in[7]; const u16* wu=(const u16*)d_in[8]; const u16* wd=(const u16*)d_in[9];
  char* ws=(char*)d_ws;
  u16* xn=(u16*)ws; u16* kv=(u16*)(ws+(2<<20)); float* y=(float*)(ws+(4<<20)); u16* H=(u16*)(ws+(8<<20));
  k_ln<<<S,64,0,stream>>>(q,k,v,sk,xn,kv);
  k_conv<<<dim3(D/64,S/4),256,0,stream>>>(kv,h,y);
  k_g1<<<dim3(F/64,S/4),256,0,stream>>>(xn,wg,wu,H);
  k_g2<<<dim3(D/64,S/4),256,0,stream>>>(H,wd,q,xn,kv,y,Dv,(u16*)d_out);
}
```

```cpp
#include <hip/hip_runtime.h>
#include <hip/hip_bf16.h>
#include <stdint.h>
namespace v0 {
typedef unsigned short u16;
typedef unsigned v4u __attribute__((ext_vector_type(4)));
typedef unsigned v2u __attribute__((ext_vector_type(2)));
typedef int v4i __attribute__((ext_vector_type(4)));
typedef short s4 __attribute__((ext_vector_type(4)));
typedef short s8 __attribute__((ext_vector_type(8)));
typedef __bf16 b8 __attribute__((ext_vector_type(8)));
typedef __bf16 b2 __attribute__((ext_vector_type(2)));
typedef float f2 __attribute__((ext_vector_type(2)));
typedef float f4 __attribute__((ext_vector_type(4)));
typedef float f16v __attribute__((ext_vector_type(16)));
#define LAS __attribute__((address_space(3)))
#define DEV __device__ __forceinline__
constexpr int S=2048, D=512, F=1792;
constexpr size_t WS_U=1u<<20, WS_Y=3u<<20, WS_H=5u<<20, WS_XN=13u<<20;
DEV float blo(unsigned u){ return __uint_as_float(u<<16); }
DEV float bhi(unsigned u){ return __uint_as_float(u&0xffff0000u); }
DEV unsigned pk2(float a,float b){ f2 v={a,b}; return __builtin_bit_cast(unsigned,__builtin_convertvector(v,b2)); }
DEV v4i mksrd(const void* p, unsigned bytes){ unsigned long long a=(unsigned long long)p; v4i r; r.x=__builtin_amdgcn_readfirstlane((int)(unsigned)a); r.y=__builtin_amdgcn_readfirstlane((int)(unsigned)(a>>32)&0xffff); r.z=bytes; r.w=0x00020000; return r; }
DEV void dma(v4i srd, int voff, int soff, int m0v){
  asm volatile("s_mov_b32 m0, %3\n\ts_nop 0\n\tbuffer_load_dwordx4 %0, %1, %2 offen lds"::"v"(voff),"s"(srd),"s"(soff),"s"(m0v):"memory"); }
#define WAITV(n) asm volatile("s_waitcnt vmcnt(%0)"::"n"(n):"memory")
#define WAITL0() asm volatile("s_waitcnt lgkmcnt(0)":::"memory")
#define BAR() do{ asm volatile("s_waitcnt lgkmcnt(0)":::"memory"); __builtin_amdgcn_s_barrier(); asm volatile("":::"memory"); }while(0)
DEV s8 trfrag(LAS char* L,int a0,int a1){ s4 x=__builtin_amdgcn_ds_read_tr16_b64_v4i16((LAS s4*)(L+a0)); s4 y=__builtin_amdgcn_ds_read_tr16_b64_v4i16((LAS s4*)(L+a1)); return __builtin_shufflevector(x,y,0,1,2,3,4,5,6,7); }
DEV f16v mm32(s8 a,s8 b,f16v c){ return __builtin_amdgcn_mfma_f32_32x32x16_bf16(__builtin_bit_cast(b8,a),__builtin_bit_cast(b8,b),c,0,0,0); }
DEV f4 mm16(s8 a,s8 b,f4 c){ return __builtin_amdgcn_mfma_f32_16x16x32_bf16(__builtin_bit_cast(b8,a),__builtin_bit_cast(b8,b),c,0,0,0); }

__global__ __launch_bounds__(256) void k0(const u16* x,const u16* k,const u16* v,const u16* sk,char* ws){
  __shared__ __attribute__((aligned(16))) u16 T[64*4*16+64*8];
  const int tid=threadIdx.x, lane=tid&63, w=tid>>6, b=blockIdx.x;
  const int tau=b>>3, cg=b&7; const int cch=tid&7, rg=tid>>3;
  const int r0=tau*64+2*rg; const u16* kp=k+cg*64+cch*8;
  v4u kr[5];
  #pragma unroll
  for(int i=0;i<5;i++){ int r=r0-3+i; kr[i]= r>=0? *(const v4u*)(kp+(size_t)r*D) : v4u{0,0,0,0}; }
  v4u vr[2]; vr[0]=*(const v4u*)(v+(size_t)r0*D+cg*64+cch*8); vr[1]=*(const v4u*)(v+(size_t)(r0+1)*D+cg*64+cch*8);
  v4u sw[4];
  #pragma unroll
  for(int i=0;i<4;i++) sw[i]=*(const v4u*)(sk+(cg*64+cch*8)*4+i*8);
  v4u xr[2];
  #pragma unroll
  for(int i=0;i<2;i++) xr[i]=*(const v4u*)(x+(size_t)(b*8+w*2+i)*D+lane*8);
  #pragma unroll
  for(int c=0;c<8;c++){
    unsigned w01=sw[c>>1][(c&1)*2], w23=sw[c>>1][(c&1)*2+1];
    float t0=blo(w01),t1=bhi(w01),t2=blo(w23),t3=bhi(w23);
    float kk[5];
    #pragma unroll
    for(int i=0;i<5;i++) kk[i]=(c&1)? bhi(kr[i][c>>1]) : blo(kr[i][c>>1]);
    #pragma unroll
    for(int i=0;i<2;i++){
      float s=kk[i]*t3+kk[i+1]*t2+kk[i+2]*t1+kk[i+3]*t0;
      float vv=(c&1)? bhi(vr[i][c>>1]) : blo(vr[i][c>>1]);
      int tl=2*rg+i;
      T[(cch*8+c)*72+(tl&3)*16+(15-(tl>>2))]=(u16)(pk2(s*vv,0.f)&0xffff);
    }
  }
  #pragma unroll
  for(int i=0;i<2;i++){
    float e[8];
    #pragma unroll
    for(int j=0;j<4;j++){ e[2*j]=blo(xr[i][j]); e[2*j+1]=bhi(xr[i][j]); }
    float s=0; for(int j=0;j<8;j++) s+=e[j];
    for(int o=32;o>0;o>>=1) s+=__shfl_xor(s,o);
    float m=s*(1.f/D); float q=0; for(int j=0;j<8;j++){ e[j]-=m; q+=e[j]*e[j]; }
    for(int o=32;o>0;o>>=1) q+=__shfl_xor(q,o);
    float r=rsqrtf(q*(1.f/D)+1e-5f);
    v4u o4; for(int j=0;j<4;j++) o4[j]=pk2(e[2*j]*r,e[2*j+1]*r);
    *(v4u*)((u16*)(ws+WS_XN)+(size_t)(b*8+w*2+i)*D+lane*8)=o4;
  }
  __syncthreads();
  #pragma unroll
  for(int i=0;i<2;i++){ int n=tid+256*i; int ch=n>>3, r=(n>>1)&3, hf=n&1;
    v4u val=*(const v4u*)(T+ch*72+r*16+hf*8);
    *(v4u*)((u16*)(ws+WS_U)+((size_t)(cg*64+ch)*4+r)*512+(496-16*tau)+hf*8)=val; }
}

constexpr int CV_H=0, CV_HS=4128+0, CV_U=2*4128, CV_US=1152, CV_Z=CV_U+8*1152, CV_ZS=(2048+8)*4;
DEV void conv2(LAS char* L,const u16* hk,const u16* Dv,char* ws,int d0){
  const int tid=threadIdx.x, lane=tid&63, w=__builtin_amdgcn_readfirstlane(tid>>6);
  #pragma unroll
  for(int i=0;i<2;i++){
    v4u hv=*(const v4u*)(hk+(size_t)(d0+i)*S+tid*8);
    if(tid==0){ float t=blo(hv[0])+__uint_as_float((unsigned)Dv[d0+i]<<16); hv[0]=(hv[0]&0xffff0000u)|(pk2(t,0.f)&0xffff); }
    *(LAS v4u*)(L+CV_H+i*CV_HS+32+tid*16)=hv;
    v4u uv=*(const v4u*)((const u16*)(ws+WS_U)+(size_t)(d0+i)*2048+tid*8);
    *(LAS v4u*)(L+CV_U+(i*4+(tid>>6))*CV_US+(tid&63)*16)=uv;
  }
  if(tid<4) *(LAS v4u*)(L+CV_H+(tid>>1)*CV_HS+(tid&1)*16)=v4u{0,0,0,0};
  if(tid>=64&&tid<128){ int n=tid-64; *(LAS v4u*)(L+CV_U+(n>>3)*CV_US+1024+(n&7)*16)=v4u{0,0,0,0}; }
  __syncthreads();
  const int ch=w>>1, r0=(w&1)*2, g=lane>>4, n=lane&15, e=(lane&15)>>2, p=lane&3;
  const int ha=CV_H+ch*CV_HS+2*(4+32*g+4*e+4*p);
  s8 A[16];
  #pragma unroll
  for(int q=0;q<16;q++) A[q]=trfrag(L,ha+256*q,ha+256*q+32);
  f4 z[2][8];
  #pragma unroll
  for(int r=0;r<2;r++) for(int i=0;i<8;i++) z[r][i]=f4{0,0,0,0};
  const int ua=CV_U+(ch*4+r0)*CV_US+2*(508-4*n+8*g);
  #pragma unroll
  for(int m=-1;m<=14;m++){
    #pragma unroll
    for(int r=0;r<2;r++){
      s4 lo=*(LAS s4*)(L+ua+r*CV_US-64*m), hi=*(LAS s4*)(L+ua+r*CV_US-64*m+8);
      s8 B=__builtin_shufflevector(lo,hi,0,1,2,3,4,5,6,7);
      #pragma unroll
      for(int i=0;i<8;i++){ int q=2*i-m; if(q>=0&&q<16&&q<=2*i+1) z[r][i]=mm16(A[q],B,z[r][i]); }
    }
  }
  const int src=4*(lane>=16? lane-16 : (lane==0?63:lane+47));
  LAS char* Z=L+CV_Z+w*CV_ZS + (w&1)*8;
  #pragma unroll
  for(int i=0;i<8;i++){
    float sv=(lane==63)? (i>0? z[1][i>0?i-1:0][3]:0.f) : z[1][i][3];
    float pv=__int_as_float(__builtin_amdgcn_ds_bpermute(src,__float_as_int(sv)));
    f2 a={z[0][i][0]+pv, z[0][i][1]+z[1][i][0]}, bb={z[0][i][2]+z[1][i][1], z[0][i][3]+z[1][i][2]};
    int o=4*(256*i+16*n+4*g);
    *(LAS f2*)(Z+o)=a; *(LAS f2*)(Z+o+8)=bb;
  }
  if((w&1)&&lane==0) *(LAS f2*)(L+CV_Z+w*CV_ZS)=f2{0,0};
  __syncthreads();
  #pragma unroll
  for(int i=0;i<2;i++){ int t=((w&1)*128+i*64+lane)*8; LAS char* za=L+CV_Z+(ch*2)*CV_ZS+4*t; LAS char* zb=za+CV_ZS;
    f4 a0=*(LAS f4*)za,a1=*(LAS f4*)(za+16),b0=*(LAS f4*)zb,b1=*(LAS f4*)(zb+16); a0+=b0; a1+=b1;
    v4u o={pk2(a0[0],a0[1]),pk2(a0[2],a0[3]),pk2(a1[0],a1[1]),pk2(a1[2],a1[3])};
    *(v4u*)((u16*)(ws+WS_Y)+(size_t)(d0+ch)*S+t)=o; }
}

constexpr int XS=16640, WSL=33792;
__global__ __launch_bounds__(256,1) void k1(const u16* wg,const u16* wu,const u16* hk,const u16* Dv,char* ws){
  extern __shared__ __attribute__((aligned(16))) char smem[];
  LAS char* L=(LAS char*)smem;
  const int tid=threadIdx.x, lane=tid&63, w=__builtin_amdgcn_readfirstlane(tid>>6);
  const int b=blockIdx.x, xcd=b&7, idx=b>>3;
  if(idx<28){
    const int rbl=idx/7, chl=idx%7, rb=4*(xcd>>1)+rbl, ch=7*(xcd&1)+chl;
    const int s=(2*rbl+((rb>>2)&1)+ch)&7;
    v4i sx=mksrd(ws+WS_XN,S*D*2), sg=mksrd(wg,D*F*2), su=mksrd(wu,D*F*2);
    const int vx=(4*(lane>>3))*1024+(lane&7)*16, vw=(4*(lane>>3))*3584+(lane&7)*16;
    const int XB=0, WB=3*XS;
    auto ld=[&](int c){ int kc=(c+s)&7, sl=c%3;
      #pragma unroll
      for(int i=0;i<4;i++) dma(sx,vx,(rb*128+32*w+i)*1024+kc*128, XB+sl*XS+(w*4+i)*1040);
      #pragma unroll
      for(int hf=0;hf<2;hf++)
      #pragma unroll
      for(int st=0;st<2;st++){ int so=(kc*64+st*32+w)*3584+ch*256+hf*128, lo=WB+sl*WSL+hf*16896+st*8448;
        dma(sg,vw,so,lo+w*1056); dma(su,vw,so,lo+(w+4)*1056);} };
    f16v acc[8];
    #pragma unroll
    for(int p=0;p<8;p++) for(int i=0;i<16;i++) acc[p][i]=0.f;
    const int r=lane&31,h=lane>>5;
    const int xa=XB+(w*4+(r&3))*1040+(r>>2)*128+16*h;
    const int grp=lane>>4, e=(lane&15)>>2, pp=lane&3;
    const int wa=WB+(e+4*(grp&1))*1056+256*(grp>>1)+8*pp;
    ld(0); ld(1);
    #pragma unroll
    for(int c=0;c<8;c++){
      if(c<7) WAITV(12); else WAITV(0);
      BAR();
      if(c+2<8) ld(c+2);
      const int sl=c%3;
      #pragma unroll
      for(int ks=0;ks<4;ks++){
        s8 xf=*(LAS s8*)(L+xa+sl*XS+32*ks);
        #pragma unroll
        for(int p=0;p<8;p++){ int o=wa+sl*WSL+(p>>2)*16896+(ks>>1)*8448+512*(ks&1)+32*(p&3);
          acc[p]=mm32(trfrag(L,o,o+128),xf,acc[p]); }
      }
    }
    const float CA=-2.f*0.7978845608028654f*1.4426950408889634f, CB=CA*0.044715f;
    #pragma unroll
    for(int p=0;p<8;p++){ float o[8];
      #pragma unroll
      for(int j=0;j<8;j++){ float g=acc[p][j],u=acc[p][8+j]; float z=g*(CA+CB*g*g); o[j]=g*u*__builtin_amdgcn_rcpf(1.f+__builtin_amdgcn_exp2f(z)); }
      v4u o4={pk2(o[0],o[1]),pk2(o[2],o[3]),pk2(o[4],o[5]),pk2(o[6],o[7])};
      *(v4u*)(ws+WS_H+((size_t)((4*rb+w)*112+8*ch+p)*64+lane)*16)=o4; }
    BAR();
  }
  conv2(L,hk,Dv,ws,2*b);
}

constexpr int KS=2176, RING=14;
__global__ __launch_bounds__(256,1) void k2(const u16* x,const u16* wd,char* ws,u16* out){
  extern __shared__ __attribute__((aligned(16))) char smem[];
  LAS char* L=(LAS char*)smem;
  const int tid=threadIdx.x, lane=tid&63, w=__builtin_amdgcn_readfirstlane(tid>>6);
  const int b=blockIdx.x, xcd=b&7, idx=b>>3;
  const int tau=8*(xcd>>1)+(idx>>2), cg=4*(xcd&1)+(idx&3);
  const int rot=(((idx>>2)+2*(idx&3))&7)*7/2;
  v4i sd=mksrd(wd,F*D*2);
  const __amdgpu_buffer_rsrc_t rh=__builtin_amdgcn_make_buffer_rsrc((void*)(ws+WS_H),0,64*112*1024,0x00020000);
  const int lam=lane>>3;
  const int vd=(4*(lam>>1)+(lam&1))*1024+(lane&7)*16+cg*128;
  const int WBASE=w*RING*KS;
  auto kidx=[&](int i){ int t=i+rot; t-= (t>=28)?28:0; return 4*t+w; };
  auto ldw=[&](int i){ int k=kidx(i); dma(sd,vd,k*16*1024,WBASE+(i%RING)*KS); dma(sd,vd,(k*16+2)*1024,WBASE+(i%RING)*KS+1088); };
  auto ldh=[&](int i,int tt){ return __builtin_bit_cast(s8,__builtin_amdgcn_raw_buffer_load_b128(rh,lane*16,((2*tau+tt)*112+kidx(i))*1024,0)); };
  constexpr int LA=6;
  s8 hf[28][2];
  #pragma unroll
  for(int i=0;i<LA;i++){ ldw(i); hf[i][0]=ldh(i,0); hf[i][1]=ldh(i,1); }
  const int YB=4*RING*KS;
  v4u yv[2];
  #pragma unroll
  for(int i=0;i<2;i++){ int n=tid+256*i; yv[i]=*(const v4u*)((const u16*)(ws+WS_Y)+(size_t)(cg*64+(n>>3))*S+tau*64+(n&7)*8); }
  f16v acc[2][2];
  #pragma unroll
  for(int a=0;a<2;a++) for(int c=0;c<2;c++) for(int i=0;i<16;i++) acc[a][c][i]=0.f;
  const int grp=lane>>4,e=(lane&15)>>2,pp=lane&3,h=lane>>5;
  const int wa=WBASE+(e>>1)*1088+256*h+128*(e&1)+32*(grp&1)+8*pp;
  #pragma unroll
  for(int i=0;i<28;i++){
    if(i+LA<28){ ldw(i+LA); hf[i+LA][0]=ldh(i+LA,0); hf[i+LA][1]=ldh(i+LA,1); }
    { constexpr int dummy=0; (void)dummy; int later=(i+LA<28? LA: 27-i);
      switch(later){ case 6: WAITV(24); break; case 5: WAITV(20); break; case 4: WAITV(16); break; case 3: WAITV(12); break; case 2: WAITV(8); break; case 1: WAITV(4); break; default: WAITV(0); } }
    int o=wa+(i%RING)*KS;
    s8 a0=trfrag(L,o,o+512), a1=trfrag(L,o+64,o+64+512);
    acc[0][0]=mm32(a0,hf[i][0],acc[0][0]); acc[1][0]=mm32(a0,hf[i][1],acc[1][0]);
    acc[0][1]=mm32(a1,hf[i][0],acc[0][1]); acc[1][1]=mm32(a1,hf[i][1],acc[1][1]);
  }
  #pragma unroll
  for(int i=0;i<2;i++){ int n=tid+256*i; *(LAS v4u*)(L+YB+(n>>3)*144+(n&7)*16)=yv[i]; }
  BAR();
  #pragma unroll
  for(int q=0;q<4;q++){ if(q!=w){ int slot=(w-q-1)&3; f16v a=acc[q&1][q>>1];
    #pragma unroll
    for(int j=0;j<4;j++) *(LAS f4*)(L+((q*3+slot)*4+j)*1024+lane*16)=f4{a[4*j],a[4*j+1],a[4*j+2],a[4*j+3]}; } }
  f16v mine= w==0?acc[0][0]: w==1?acc[1][0]: w==2?acc[0][1]:acc[1][1];
  BAR();
  #pragma unroll
  for(int sl=0;sl<3;sl++)
  #pragma unroll
  for(int j=0;j<4;j++){ f4 t=*(LAS f4*)(L+((w*3+sl)*4+j)*1024+lane*16); for(int c=0;c<4;c++) mine[4*j+c]+=t[c]; }
  const int tt=w&1, dt=w>>1, tok=tau*64+32*tt+(lane&31);
  #pragma unroll
  for(int q=0;q<4;q++){ int dl=32*dt+8*q+4*h;
    s4 y4=__builtin_amdgcn_ds_read_tr16_b64_v4i16((LAS s4*)(L+YB+(dl+e)*144+(32*tt+16*(grp&1)+4*pp)*2));
    v2u yy=__builtin_bit_cast(v2u,y4);
    size_t off=(size_t)tok*D+cg*64+dl;
    v2u xv=*(const v2u*)(x+off), xn=*(const v2u*)((const u16*)(ws+WS_XN)+off);
    float o0=blo(xv[0])+blo(xn[0])*blo(yy[0])+mine[4*q], o1=bhi(xv[0])+bhi(xn[0])*bhi(yy[0])+mine[4*q+1];
    float o2=blo(xv[1])+blo(xn[1])*blo(yy[1])+mine[4*q+2], o3=bhi(xv[1])+bhi(xn[1])*bhi(yy[1])+mine[4*q+3];
    *(v2u*)(out+off)=v2u{pk2(o0,o1),pk2(o2,o3)};
  }
}
}
extern "C" void kernel_launch(void* const* d_in, const int* in_sizes, int n_in, void* d_out, int out_size, void* d_ws, size_t ws_size, hipStream_t stream){
  using namespace v0;
  const u16* q=(const u16*)d_in[0]; char* ws=(char*)d_ws;
  hipFuncSetAttribute((const void*)k1,hipFuncAttributeMaxDynamicSharedMemorySize,3*XS+3*WSL);
  hipFuncSetAttribute((const void*)k2,hipFuncAttributeMaxDynamicSharedMemorySize,4*RING*KS+64*144);
  k0<<<256,256,0,stream>>>(q,(const u16*)d_in[1],(const u16*)d_in[2],(const u16*)d_in[3],ws);
  k1<<<256,256,3*XS+3*WSL,stream>>>((const u16*)d_in[7],(const u16*)d_in[8],(const u16*)d_in[4],(const u16*)d_in[5],ws);
  k2<<<256,256,4*RING*KS+64*144,stream>>>(q,(const u16*)d_in[9],ws,(u16*)d_out);
}
```

```cpp
#include <hip/hip_runtime.h>
#include <stdint.h>
namespace mk {
typedef unsigned short u16;
typedef unsigned long long u64;
typedef unsigned v4u __attribute__((ext_vector_type(4)));
typedef unsigned v2u __attribute__((ext_vector_type(2)));
typedef __bf16 b2 __attribute__((ext_vector_type(2)));
typedef float f2 __attribute__((ext_vector_type(2)));
#define LAS __attribute__((address_space(3)))
#define GAS __attribute__((address_space(1)))
#define DEV __device__ __forceinline__
#define RLX __ATOMIC_RELAXED, __HIP_MEMORY_SCOPE_AGENT
constexpr unsigned PFR = 0x1000, KF = 0x4000, UO = 0x100000, WSZ = 0xC00000;
constexpr u64 TAGK = 0x9E3779B97F4A7C15ull;
constexpr int LDS_BYTES = 163840;
DEV float blo(unsigned u) { return __uint_as_float(u << 16); }
DEV float bhi(unsigned u) { return __uint_as_float(u & 0xffff0000u); }
DEV unsigned pk2(float a, float b) { f2 v = {a, b}; return __builtin_bit_cast(unsigned, __builtin_convertvector(v, b2)); }

DEV u64 kv_job(LAS char* L, const u16* k, const u16* v, const u16* sk, char* ws, int j) {
  const int tid = threadIdx.x, cch = tid & 7, rg = tid >> 3, cg = j & 7, q = j >> 3;
  const int r0 = q * 512 + rg * 16, col = cg * 64 + cch * 8;
  v4u kr[19], vr[16], sw[4];
  unsigned touch; u64 pc;
  asm volatile("s_getpc_b64 %1\n\tglobal_load_dword %0, %2, %1" : "=&v"(touch), "=&s"(pc) : "v"(((j & 3) * 16 + (tid >> 6) * 4 + (tid & 3)) * 128));
#pragma unroll
  for (int i = 0; i < 4; i++) sw[i] = *(const GAS v4u*)(sk + col * 4 + i * 8);
#pragma unroll
  for (int i = 0; i < 19; i++) {
    const int r = r0 - 3 + i;
    kr[i] = *(const GAS v4u*)(k + (size_t)(r < 0 ? 0 : r) * 512 + col);
    if (i >= 3) vr[i - 3] = *(const GAS v4u*)(v + (size_t)r * 512 + col);
  }
  const u64 word = __hip_atomic_load((GAS u64*)ws, RLX);
  __builtin_amdgcn_sched_barrier(0);
  auto un = [](unsigned u) { return f2{blo(u), bhi(u)}; };
  f2 w[4][4], kf[4][4];
#pragma unroll
  for (int c = 0; c < 4; c++) {
    w[0][c] = f2{blo(sw[c][0]), blo(sw[c][2])}; w[1][c] = f2{bhi(sw[c][0]), bhi(sw[c][2])};
    w[2][c] = f2{blo(sw[c][1]), blo(sw[c][3])}; w[3][c] = f2{bhi(sw[c][1]), bhi(sw[c][3])};
  }
  unsigned o[16][4];
#pragma unroll
  for (int i = 0; i < 19; i++) {
#pragma unroll
    for (int c = 0; c < 4; c++) {
      kf[i & 3][c] = un((i < 3 && r0 == 0) ? 0u : kr[i][c]);
      if (i >= 3) {
        const f2 s = (kf[(i + 1) & 3][c] * w[3][c] + kf[(i + 2) & 3][c] * w[2][c] + kf[(i + 3) & 3][c] * w[1][c] + kf[i & 3][c] * w[0][c]) * un(vr[i - 3][c]);
        o[i - 3][c] = pk2(s[0], s[1]);
      }
    }
    __builtin_amdgcn_sched_barrier(0);
  }
  const int pos = cch * 8192 + (((15 - (rg >> 1)) ^ cch) << 4) + ((~rg & 1) << 3);
#pragma unroll
  for (int c = 0; c < 4; c++)
#pragma unroll
    for (int r = 0; r < 4; r++) {
      const v2u e = {__builtin_amdgcn_perm(o[8 + r][c], o[12 + r][c], 0x05040100u), __builtin_amdgcn_perm(o[r][c], o[4 + r][c], 0x05040100u)};
      const v2u d = {__builtin_amdgcn_perm(o[8 + r][c], o[12 + r][c], 0x07060302u), __builtin_amdgcn_perm(o[r][c], o[4 + r][c], 0x07060302u)};
      *(LAS v2u*)(L + pos + 2 * c * 1024 + r * 256) = e;
      *(LAS v2u*)(L + pos + (2 * c + 1) * 1024 + r * 256) = d;
    }
  __syncthreads();
  const __amdgpu_buffer_rsrc_t rs = __builtin_amdgcn_make_buffer_rsrc(ws, 0, WSZ, 0x00020000);
#pragma unroll
  for (int i = 0; i < 16; i++) {
    const int n = tid + 256 * i, seg = n >> 4, jj = n & 15;
    const v4u val = *(LAS v4u*)(L + seg * 256 + ((jj ^ (seg >> 5)) << 4));
    __builtin_amdgcn_raw_buffer_store_b128(val, rs, UO + (cg * 256 + seg) * 1024 + (384 - 128 * q) * 2 + jj * 16, 0, 16);
  }
  asm volatile("s_waitcnt vmcnt(0)" ::"v"(touch) : "memory");
  __syncthreads();
  u64 tag = word + TAGK;
  tag = ((u64)(unsigned)__builtin_amdgcn_readfirstlane((int)(tag >> 32)) << 32) | (unsigned)__builtin_amdgcn_readfirstlane((int)tag);
  if (tid == 0) __hip_atomic_store((GAS u64*)(ws + KF + cg * 128 + q * 8), tag, RLX);
  return tag;
}

__global__ __launch_bounds__(256, 1) void fused(const u16* x, const u16* hk, const u16* dv, const u16* wg, const u16* wu, const u16* wd,
                                                u16* out, char* ws, const u16* k, const u16* v, const u16* sk) {
  extern __shared__ __attribute__((aligned(16))) char smem[];
  const unsigned b = blockIdx.x;
  u64 tag = 0;
  if ((b >> 3) >= 28) {
    tag = kv_job((LAS char*)smem, k, v, sk, ws, (int)((b & 7) * 4 + (b >> 3) - 28));
    if (b == 255 && threadIdx.x < 64) {
      const int l = threadIdx.x;
      for (unsigned spins = 0; spins < (1u << 18); ++spins) {
        bool ok = __hip_atomic_load((GAS u64*)(ws + KF + (l & 7) * 128 + ((l >> 3) & 3) * 8), RLX) == tag;
#pragma unroll
        for (int i = 0; i < 4; i++) {
          const int n = min(l + 64 * i, 223);
          ok &= __hip_atomic_load((GAS u64*)(ws + PFR + (n / 14) * 128 + (n % 14) * 8), RLX) == tag;
        }
        if (__all(ok)) break;
        __builtin_amdgcn_s_sleep(4);
      }
      if (l == 0) __hip_atomic_store((GAS u64*)ws, tag, RLX);
    }
  }
  asm volatile(
    "s_load_dwordx2 s[36:37], %[ka], 0x0\n"
    "s_load_dwordx2 s[40:41], %[ka], 0x18\n"
    "s_load_dwordx2 s[44:45], %[ka], 0x20\n"
    "s_load_dwordx2 s[60:61], %[ka], 0x38\n"
    "s_load_dwordx2 s[52:53], %[ka], 0x8\n"
    "s_load_dwordx2 s[20:21], %[ka], 0x10\n"
    "s_load_dwordx2 s[48:49], %[ka], 0x28\n"
    "s_load_dwordx2 s[56:57], %[ka], 0x30\n"
    "v_and_b32 v1, 63, %[tid]\n"
    "v_lshrrev_b32 v44, 6, %[tid]\n"
    "s_mov_b32 s13, %[bid]\n"
    "v_readfirstlane_b32 s12, v44\n"
    "s_and_b32 s14, s13, 7\n"
    "s_lshr_b32 s15, s13, 3\n"
    "s_cmp_ge_u32 s15, 28\n"
    "s_cbranch_scc1 .LB_%=\n"
    "s_mul_i32 s67, s15, 37\n"
    "s_lshr_b32 s67, s67, 8\n"
    "s_mul_i32 s16, s67, 7\n"
    "s_sub_u32 s16, s15, s16\n"
    "s_lshr_b32 s17, s14, 1\n"
    "s_lshl_b32 s17, s17, 2\n"
    "s_add_u32 s64, s17, s67\n"
    "s_and_b32 s17, s14, 1\n"
    "s_mul_i32 s17, s17, 7\n"
    "s_add_u32 s65, s17, s16\n"
    "s_lshr_b32 s17, s64, 2\n"
    "s_and_b32 s17, s17, 1\n"
    "s_lshl_b32 s18, s67, 1\n"
    "s_add_u32 s17, s17, s18\n"
    "s_add_u32 s17, s17, s65\n"
    "s_and_b32 s66, s17, 7\n"
    "v_lshrrev_b32 v45, 3, v1\n"
    "v_and_b32 v46, 7, v1\n"
    "v_lshlrev_b32 v46, 4, v46\n"
    "s_lshl_b32 s17, s12, 15\n"
    "s_add_u32 s17, s17, 64\n"
    "v_lshl_add_u32 v4, v45, 12, v46\n"
    "v_add_u32 v4, s17, v4\n"
    "v_subrev_u32 v5, 16, v4\n"
    "v_subrev_u32 v6, 32, v4\n"
    "v_subrev_u32 v7, 48, v4\n"
    "s_mul_i32 s17, s12, 0xe00\n"
    "s_movk_i32 s18, 0x3800\n"
    "v_mad_u32_u24 v9, v45, s18, v46\n"
    "v_add_u32 v9, s17, v9\n"
    "v_add_u32 v8, 0x480, v9\n"
    "v_add_u32 v10, 0x1bc00, v9\n"
    "v_add_u32 v11, 0x1b780, v9\n"
    "s_mul_i32 s88, s12, 0x1040\n"
    "s_mul_i32 s89, s12, 0x1120\n"
    "s_add_u32 s89, s89, 0xc300\n"
    "s_lshl_b32 s17, s64, 17\n"
    "s_lshr_b32 s18, s64, 3\n"
    "s_lshl_b32 s18, s18, 7\n"
    "s_lshl_b32 s19, s65, 8\n"
    "s_add_u32 s18, s18, s19\n"
    "s_add_u32 s19, s66, 0\n"
    "s_and_b32 s19, s19, 7\n"
    "s_lshl_b32 s72, s19, 7\n"
    "s_add_u32 s72, s72, s17\n"
    "s_mul_i32 s80, s19, 0x38000\n"
    "s_add_u32 s80, s80, s18\n"
    "s_waitcnt lgkmcnt(0)\n"
    "s_sub_u32 s36, s36, 64\n"
    "s_subb_u32 s37, s37, 0\n"
    "s_and_b32 s37, s37, 0xffff\n"
    "s_mov_b32 s38, 0x200040\n"
    "s_mov_b32 s39, 0x20000\n"
    "s_sub_u32 s40, s40, 0x480\n"
    "s_subb_u32 s41, s41, 0\n"
    "s_and_b32 s41, s41, 0xffff\n"
    "s_mov_b32 s42, 0x1c0480\n"
    "s_mov_b32 s43, 0x20000\n"
    "s_sub_u32 s44, s44, 0x480\n"
    "s_subb_u32 s45, s45, 0\n"
    "s_and_b32 s45, s45, 0xffff\n"
    "s_mov_b32 s46, 0x1c0480\n"
    "s_mov_b32 s47, 0x20000\n"
    "s_add_u32 m0, s88, 0\n"
    "s_nop 0\n"
    "buffer_load_dwordx4 v4, s[36:39], s72 offen lds\n"
    "buffer_load_dwordx4 v5, s[36:39], s72 offen offset:1040 lds\n"
    "buffer_load_dwordx4 v6, s[36:39], s72 offen offset:2080 lds\n"
    "buffer_load_dwordx4 v7, s[36:39], s72 offen offset:3120 lds\n"
    "s_add_u32 m0, s89, 0\n"
    "s_nop 0\n"
    "buffer_load_dwordx4 v8, s[40:43], s80 offen lds\n"
    "buffer_load_dwordx4 v9, s[44:47], s80 offen offset:1152 lds\n"
    "buffer_load_dwordx4 v10, s[40:43], s80 offen offset:2176 lds\n"
    "buffer_load_dwordx4 v11, s[44:47], s80 offen offset:3328 lds\n"
    "s_and_b32 s61, s61, 0xffff\n"
    "s_mov_b32 s62, 0xc00000\n"
    "s_mov_b32 s63, 0x20000\n"
    "s_and_b32 s53, s53, 0xffff\n"
    "s_mov_b32 s54, 0x200000\n"
    "s_mov_b32 s55, 0x20000\n"
    "s_lshl_b32 s19, s13, 13\n"
    "s_lshr_b32 s33, s12, 1\n"
    "s_lshl_b32 s33, s33, 12\n"
    "s_add_u32 s19, s19, s33\n"
    "s_lshl_b32 s33, s13, 2\n"
    "v_mov_b32 v15, 0\n"
    "v_mov_b32 v49, s33\n"
    "buffer_load_dword v20, v15, s[52:55], s19 offen\n"
    "global_load_dword v21, v49, s[20:21]\n"
    "buffer_load_dwordx2 v[254:255], v15, s[60:63], 0 offen\n"
    "s_getpc_b64 s[10:11]\n"
    ".Lpc_%=:\n"
    "s_lshl_b32 s19, s15, 2\n"
    "s_add_u32 s19, s19, s12\n"
    "s_lshl_b32 s19, s19, 8\n"
    "v_and_b32 v47, 1, v1\n"
    "v_lshlrev_b32 v47, 7, v47\n"
    "v_add_u32 v47, s19, v47\n"
    "v_min_u32 v47, .Lend_%=-.Lpc_%=-4, v47\n"
    "global_load_dword v48, v47, s[10:11]\n"
    "s_add_u32 s19, s66, 1\n"
    "s_and_b32 s19, s19, 7\n"
    "s_lshl_b32 s73, s19, 7\n"
    "s_add_u32 s73, s73, s17\n"
    "s_mul_i32 s81, s19, 0x38000\n"
    "s_add_u32 s81, s81, s18\n"
    "s_add_u32 m0, s88, 0x4100\n"
    "s_nop 0\n"
    "buffer_load_dwordx4 v4, s[36:39], s73 offen lds\n"
    "buffer_load_dwordx4 v5, s[36:39], s73 offen offset:1040 lds\n"
    "buffer_load_dwordx4 v6, s[36:39], s73 offen offset:2080 lds\n"
    "buffer_load_dwordx4 v7, s[36:39], s73 offen offset:3120 lds\n"
    "s_add_u32 m0, s89, 0x4480\n"
    "s_nop 0\n"
    "buffer_load_dwordx4 v8, s[40:43], s81 offen lds\n"
    "buffer_load_dwordx4 v9, s[44:47], s81 offen offset:1152 lds\n"
    "buffer_load_dwordx4 v10, s[40:43], s81 offen offset:2176 lds\n"
    "buffer_load_dwordx4 v11, s[44:47], s81 offen offset:3328 lds\n"
    "v_and_b32 v128, 31, v1\n"
    "v_lshrrev_b32 v129, 5, v1\n"
    "v_bfe_u32 v130, v1, 2, 2\n"
    "v_and_b32 v131, 3, v1\n"
    "v_bfe_u32 v132, v1, 4, 1\n"
    "v_and_b32 v133, 3, v128\n"
    "v_mul_u32_u24 v133, 0x410, v133\n"
    "v_lshrrev_b32 v134, 2, v128\n"
    "v_lshl_add_u32 v133, v134, 7, v133\n"
    "v_lshl_add_u32 v133, v129, 4, v133\n"
    "v_add_u32 v2, s88, v133\n"
    "v_mul_u32_u24 v133, 0x1120, v130\n"
    "v_mul_u32_u24 v134, 0x480, v132\n"
    "v_add_u32 v133, v133, v134\n"
    "v_lshl_add_u32 v133, v129, 8, v133\n"
    "v_lshl_add_u32 v133, v131, 3, v133\n"
    "v_add_u32 v3, 0xc300, v133\n"
    "s_add_u32 s19, s66, 2\n"
    "s_and_b32 s19, s19, 7\n"
    "s_lshl_b32 s74, s19, 7\n"
    "s_add_u32 s74, s74, s17\n"
    "s_mul_i32 s82, s19, 0x38000\n"
    "s_add_u32 s82, s82, s18\n"
    "s_add_u32 s19, s66, 3\n"
    "s_and_b32 s19, s19, 7\n"
    "s_lshl_b32 s75, s19, 7\n"
    "s_add_u32 s75, s75, s17\n"
    "s_mul_i32 s83, s19, 0x38000\n"
    "s_add_u32 s83, s83, s18\n"
    "s_add_u32 s19, s66, 4\n"
    "s_and_b32 s19, s19, 7\n"
    "s_lshl_b32 s76, s19, 7\n"
    "s_add_u32 s76, s76, s17\n"
    "s_mul_i32 s84, s19, 0x38000\n"
    "s_add_u32 s84, s84, s18\n"
    "s_add_u32 s19, s66, 5\n"
    "s_and_b32 s19, s19, 7\n"
    "s_lshl_b32 s77, s19, 7\n"
    "s_add_u32 s77, s77, s17\n"
    "s_mul_i32 s85, s19, 0x38000\n"
    "s_add_u32 s85, s85, s18\n"
    "s_add_u32 s19, s66, 6\n"
    "s_and_b32 s19, s19, 7\n"
    "s_lshl_b32 s78, s19, 7\n"
    "s_add_u32 s78, s78, s17\n"
    "s_mul_i32 s86, s19, 0x38000\n"
    "s_add_u32 s86, s86, s18\n"
    "s_add_u32 s19, s66, 7\n"
    "s_and_b32 s19, s19, 7\n"
    "s_lshl_b32 s79, s19, 7\n"
    "s_add_u32 s79, s79, s17\n"
    "s_mul_i32 s87, s19, 0x38000\n"
    "s_add_u32 s87, s87, s18\n"
    "s_lshl_b32 s19, s64, 14\n"
    "s_add_u32 s94, s19, 0x10000\n"
    "s_lshl_b32 s19, s65, 13\n"
    "s_lshr_b32 s16, s64, 3\n"
    "s_lshl_b32 s16, s16, 12\n"
    "s_add_u32 s19, s19, s16\n"
    "s_add_u32 s95, s19, 0x60000\n"
    "s_mov_b32 s34, 0x3f803f80\n"
    "s_lshl_b32 s19, s12, 8\n"
    "v_lshlrev_b32 v133, 3, v128\n"
    "v_add_u32 v133, s19, v133\n"
    "v_lshl_add_u32 v54, v129, 12, v133\n"
    "s_lshl_b32 s17, s65, 10\n"
    "v_add_u32 v32, s17, v133\n"
    "s_bfe_u32 s17, s64, 0x10002\n"
    "s_lshl_b32 s17, s17, 9\n"
    "s_and_b32 s18, s64, 3\n"
    "s_lshl_b32 s18, s18, 2\n"
    "s_add_u32 s17, s17, s18\n"
    "s_lshl_b32 s18, s12, 10\n"
    "s_add_u32 s17, s17, s18\n"
    "v_lshlrev_b32 v133, 4, v128\n"
    "v_add_u32 v33, s17, v133\n"
    "s_lshl_b32 s17, s12, 5\n"
    "v_add_u32 v34, s17, v3\n"
    "s_waitcnt vmcnt(8)\n"
    "s_barrier\n"
    "ds_read_b64_tr_b16 v[192:193], v3\n"
    "ds_read_b64_tr_b16 v[194:195], v3 offset:128\n"
    "ds_read_b128 a[0:3], v2\n"
    "ds_read_b64_tr_b16 v[196:197], v3 offset:32\n"
    "ds_read_b64_tr_b16 v[198:199], v3 offset:160\n"
    "ds_read_b64_tr_b16 v[200:201], v3 offset:64\n"
    "ds_read_b64_tr_b16 v[202:203], v3 offset:192\n"
    "ds_read_b64_tr_b16 v[204:205], v3 offset:96\n"
    "ds_read_b64_tr_b16 v[206:207], v3 offset:224\n"
    "ds_read_b64_tr_b16 v[208:209], v3 offset:512\n"
    "ds_read_b64_tr_b16 v[210:211], v3 offset:640\n"
    "ds_read_b128 a[4:7], v2 offset:32\n"
    "ds_read_b64_tr_b16 v[212:213], v3 offset:544\n"
    "ds_read_b64_tr_b16 v[214:215], v3 offset:672\n"
    "ds_read_b64_tr_b16 v[216:217], v3 offset:576\n"
    "ds_read_b64_tr_b16 v[218:219], v3 offset:704\n"
    "ds_read_b64_tr_b16 v[220:221], v3 offset:608\n"
    "ds_read_b64_tr_b16 v[222:223], v3 offset:736\n"
    "s_waitcnt lgkmcnt(9)\n"
    "v_mfma_f32_32x32x16_bf16 v[64:79], v[192:195], a[0:3], 0\n"
    "ds_read_b64_tr_b16 v[224:225], v3 offset:2176\n"
    "ds_read_b64_tr_b16 v[226:227], v3 offset:2304\n"
    "s_add_u32 m0, s88, 0x8200\n"
    "s_nop 0\n"
    "buffer_load_dwordx4 v4, s[36:39], s74 offen lds\n"
    "v_readfirstlane_b32 s8, v254\n"
    "v_readfirstlane_b32 s9, v255\n"
    "v_mfma_f32_32x32x16_bf16 v[80:95], v[196:199], a[0:3], 0\n"
    "ds_read_b128 a[8:11], v2 offset:64\n"
    "ds_read_b64_tr_b16 v[228:229], v3 offset:2208\n"
    "buffer_load_dwordx4 v5, s[36:39], s74 offen offset:1040 lds\n"
    "s_cmp_eq_u32 s12, 0\n"
    "s_cselect_b64 s[10:11], 3, 0\n"
    "v_mfma_f32_32x32x16_bf16 v[96:111], v[200:203], a[0:3], 0\n"
    "ds_read_b64_tr_b16 v[230:231], v3 offset:2336\n"
    "ds_read_b64_tr_b16 v[232:233], v3 offset:2240\n"
    "buffer_load_dwordx4 v6, s[36:39], s74 offen offset:2080 lds\n"
    "s_add_u32 s8, s8, 0x7f4a7c15\n"
    "s_addc_u32 s9, s9, 0x9e3779b9\n"
    "s_lshl_b32 s16, s64, 7\n"
    "v_mfma_f32_32x32x16_bf16 v[112:127], v[204:207], a[0:3], 0\n"
    "ds_read_b64_tr_b16 v[234:235], v3 offset:2368\n"
    "ds_read_b64_tr_b16 v[236:237], v3 offset:2272\n"
    "ds_read_b64_tr_b16 v[238:239], v3 offset:2400\n"
    "buffer_load_dwordx4 v7, s[36:39], s74 offen offset:3120 lds\n"
    "s_add_u32 s16, s16, 0x1000\n"
    "s_waitcnt lgkmcnt(9)\n"
    "v_mfma_f32_32x32x16_bf16 v[64:79], v[208:211], a[4:7], v[64:79]\n"
    "ds_read_b64_tr_b16 v[192:193], v3 offset:2688\n"
    "ds_read_b64_tr_b16 v[194:195], v3 offset:2816\n"
    "ds_read_b128 a[12:15], v2 offset:96\n"
    "ds_read_b64_tr_b16 v[196:197], v3 offset:2720\n"
    "ds_read_b64_tr_b16 v[198:199], v3 offset:2848\n"
    "s_add_u32 m0, s89, 0x8900\n"
    "v_mfma_f32_32x32x16_bf16 v[80:95], v[212:215], a[4:7], v[80:95]\n"
    "ds_read_b64_tr_b16 v[200:201], v3 offset:2752\n"
    "ds_read_b64_tr_b16 v[202:203], v3 offset:2880\n"
    "ds_read_b64_tr_b16 v[204:205], v3 offset:2784\n"
    "ds_read_b64_tr_b16 v[206:207], v3 offset:2912\n"
    "buffer_load_dwordx4 v8, s[40:43], s82 offen lds\n"
    "buffer_load_dwordx4 v9, s[44:47], s82 offen offset:1152 lds\n"
    "v_mfma_f32_32x32x16_bf16 v[96:111], v[216:219], a[4:7], v[96:111]\n"
    "buffer_load_dwordx4 v10, s[40:43], s82 offen offset:2176 lds\n"
    "s_lshl_b32 s19, s65, 7\n"
    "s_add_u32 s19, s19, 0x2000\n"
    "v_lshlrev_b32 v133, 3, v1\n"
    "v_add_u32 v133, s16, v133\n"
    "v_subrev_u32 v134, 8, v1\n"
    "v_mfma_f32_32x32x16_bf16 v[112:127], v[220:223], a[4:7], v[112:127]\n"
    "buffer_load_dwordx4 v11, s[44:47], s82 offen offset:3328 lds\n"
    "v_and_b32 v134, 15, v134\n"
    "v_lshlrev_b32 v134, 3, v134\n"
    "v_add_u32 v134, s19, v134\n"
    "v_cmp_gt_u32 vcc, 8, v1\n"
    "s_waitcnt lgkmcnt(9)\n"
    "v_mfma_f32_32x32x16_bf16 v[64:79], v[224:227], a[8:11], v[64:79]\n"
    "ds_read_b128 v[136:139], v2\n"
    "ds_read_b64_tr_b16 v[152:153], v34\n"
    "ds_read_b64_tr_b16 v[154:155], v34 offset:128\n"
    "ds_read_b128 v[140:143], v2 offset:32\n"
    "ds_read_b64_tr_b16 v[156:157], v34 offset:512\n"
    "ds_read_b64_tr_b16 v[158:159], v34 offset:640\n"
    "ds_read_b128 v[144:147], v2 offset:64\n"
    "ds_read_b64_tr_b16 v[160:161], v34 offset:2176\n"
    "ds_read_b64_tr_b16 v[162:163], v34 offset:2304\n"
    "ds_read_b128 v[148:151], v2 offset:96\n"
    "ds_read_b64_tr_b16 v[164:165], v34 offset:2688\n"
    "ds_read_b64_tr_b16 v[166:167], v34 offset:2816\n"
    "v_mov_b32 v168, 0\n"
    "v_mov_b32 v169, 0\n"
    "v_mov_b32 v170, 0\n"
    "v_mov_b32 v171, 0\n"
    "v_mov_b32 v172, 0\n"
    "v_mov_b32 v173, 0\n"
    "s_waitcnt lgkmcnt(11)\n"
    "v_dot2_f32_bf16 v168, v136, s34, v168\n"
    "v_dot2_f32_bf16 v170, v136, v136, v170\n"
    "s_waitcnt lgkmcnt(10)\n"
    "v_dot2_f32_bf16 v172, v152, s34, v172\n"
    "v_dot2_f32_bf16 v169, v137, s34, v169\n"
    "v_dot2_f32_bf16 v171, v137, v137, v171\n"
    "v_dot2_f32_bf16 v173, v153, s34, v173\n"
    "v_dot2_f32_bf16 v168, v138, s34, v168\n"
    "v_dot2_f32_bf16 v170, v138, v138, v170\n"
    "s_waitcnt lgkmcnt(9)\n"
    "v_dot2_f32_bf16 v172, v154, s34, v172\n"
    "v_dot2_f32_bf16 v169, v139, s34, v169\n"
    "v_dot2_f32_bf16 v171, v139, v139, v171\n"
    "v_dot2_f32_bf16 v173, v155, s34, v173\n"
    "s_waitcnt lgkmcnt(8)\n"
    "v_dot2_f32_bf16 v168, v140, s34, v168\n"
    "v_dot2_f32_bf16 v170, v140, v140, v170\n"
    "s_waitcnt lgkmcnt(7)\n"
    "v_dot2_f32_bf16 v172, v156, s34, v172\n"
    "v_dot2_f32_bf16 v169, v141, s34, v169\n"
    "v_dot2_f32_bf16 v171, v141, v141, v171\n"
    "v_dot2_f32_bf16 v173, v157, s34, v173\n"
    "v_dot2_f32_bf16 v168, v142, s34, v168\n"
    "v_dot2_f32_bf16 v170, v142, v142, v170\n"
    "s_waitcnt lgkmcnt(6)\n"
    "v_dot2_f32_bf16 v172, v158, s34, v172\n"
    "v_dot2_f32_bf16 v169, v143, s34, v169\n"
    "v_dot2_f32_bf16 v171, v143, v143, v171\n"
    "v_dot2_f32_bf16 v173, v159, s34, v173\n"
    "s_waitcnt lgkmcnt(5)\n"
    "v_dot2_f32_bf16 v168, v144, s34, v168\n"
    "v_dot2_f32_bf16 v170, v144, v144, v170\n"
    "s_waitcnt lgkmcnt(4)\n"
    "v_dot2_f32_bf16 v172, v160, s34, v172\n"
    "v_dot2_f32_bf16 v169, v145, s34, v169\n"
    "v_dot2_f32_bf16 v171, v145, v145, v171\n"
    "v_dot2_f32_bf16 v173, v161, s34, v173\n"
    "v_dot2_f32_bf16 v168, v146, s34, v168\n"
    "v_dot2_f32_bf16 v170, v146, v146, v170\n"
    "s_waitcnt lgkmcnt(3)\n"
    "v_dot2_f32_bf16 v172, v162, s34, v172\n"
    "v_dot2_f32_bf16 v169, v147, s34, v169\n"
    "v_dot2_f32_bf16 v171, v147, v147, v171\n"
    "v_dot2_f32_bf16 v173, v163, s34, v173\n"
    "s_waitcnt lgkmcnt(2)\n"
    "v_dot2_f32_bf16 v168, v148, s34, v168\n"
    "v_dot2_f32_bf16 v170, v148, v148, v170\n"
    "s_waitcnt lgkmcnt(1)\n"
    "v_dot2_f32_bf16 v172, v164, s34, v172\n"
    "v_dot2_f32_bf16 v169, v149, s34, v169\n"
    "v_dot2_f32_bf16 v171, v149, v149, v171\n"
    "v_dot2_f32_bf16 v173, v165, s34, v173\n"
    "v_dot2_f32_bf16 v168, v150, s34, v168\n"
    "v_dot2_f32_bf16 v170, v150, v150, v170\n"
    "s_waitcnt lgkmcnt(0)\n"
    "v_dot2_f32_bf16 v172, v166, s34, v172\n"
    "v_dot2_f32_bf16 v169, v151, s34, v169\n"
    "v_dot2_f32_bf16 v171, v151, v151, v171\n"
    "v_dot2_f32_bf16 v173, v167, s34, v173\n"
    "s_nop 0\n"
    "v_add_f32 v174, v168, v169\n"
    "v_add_f32 v175, v170, v171\n"
    "v_add_f32 v178, v172, v173\n"
    "v_mov_b32 v176, v174\n"
    "v_mov_b32 v177, v175\n"
    "v_mov_b32 v179, v178\n"
    "v_permlane32_swap_b32 v174, v176\n"
    "v_permlane32_swap_b32 v175, v177\n"
    "v_permlane32_swap_b32 v178, v179\n"
    "v_add_f32 v178, v178, v179\n"
    "v_add_f32 v174, v174, v176\n"
    "v_add_f32 v175, v175, v177\n"
    "v_cvt_pk_bf16_f32 v179, v178, v178\n"
    "buffer_store_dwordx2 v[174:175], v32, s[60:63], s94 offen sc1\n"
    "v_and_b32 v179, 0xffff0000, v179\n"
    "v_sub_f32 v179, v178, v179\n"
    "v_cvt_pk_bf16_f32 v178, v178, v179\n"
    "buffer_store_dword v178, v33, s[60:63], s95 offen sc1\n"
    "s_waitcnt vmcnt(10) lgkmcnt(0)\n"
    "s_barrier\n"
    "v_mfma_f32_32x32x16_bf16 v[80:95], v[228:231], a[8:11], v[80:95]\n"
    "ds_read_b64_tr_b16 v[208:209], v3 offset:17536\n"
    "ds_read_b64_tr_b16 v[210:211], v3 offset:17664\n"
    "ds_read_b128 a[16:19], v2 offset:16640\n"
    "s_add_u32 m0, s88, 0\n"
    "s_nop 0\n"
    "buffer_load_dwordx4 v4, s[36:39], s75 offen lds\n"
    "s_lshl_b32 s17, s65, 3\n"
    "v_mfma_f32_32x32x16_bf16 v[96:111], v[232:235], a[8:11], v[96:111]\n"
    "ds_read_b64_tr_b16 v[212:213], v3 offset:17568\n"
    "ds_read_b64_tr_b16 v[214:215], v3 offset:17696\n"
    "ds_read_b64_tr_b16 v[216:217], v3 offset:17600\n"
    "buffer_load_dwordx4 v5, s[36:39], s75 offen offset:1040 lds\n"
    "s_add_u32 s17, s17, s16\n"
    "v_cndmask_b32 v52, v134, v133, vcc\n"
    "v_mfma_f32_32x32x16_bf16 v[112:127], v[236:239], a[8:11], v[112:127]\n"
    "ds_read_b64_tr_b16 v[218:219], v3 offset:17728\n"
    "ds_read_b64_tr_b16 v[220:221], v3 offset:17632\n"
    "ds_read_b64_tr_b16 v[222:223], v3 offset:17760\n"
    "s_lshl_b32 s18, s64, 3\n"
    "s_add_u32 s18, s18, s19\n"
    "v_mfma_f32_32x32x16_bf16 v[64:79], v[192:195], a[12:15], v[64:79]\n"
    "ds_read_b64_tr_b16 v[224:225], v3 offset:18048\n"
    "ds_read_b64_tr_b16 v[226:227], v3 offset:18176\n"
    "buffer_load_dwordx4 v6, s[36:39], s75 offen offset:2080 lds\n"
    "v_mov_b32 v133, s17\n"
    "v_cmp_eq_u32 vcc, 0, v1\n"
    "v_mov_b32 v134, s18\n"
    "v_mfma_f32_32x32x16_bf16 v[80:95], v[196:199], a[12:15], v[80:95]\n"
    "ds_read_b128 a[20:23], v2 offset:16672\n"
    "ds_read_b64_tr_b16 v[228:229], v3 offset:18080\n"
    "s_nop 0\n"
    "v_cndmask_b32 v53, v134, v133, vcc\n"
    "v_readfirstlane_b32 s96, v20\n"
    "v_readfirstlane_b32 s97, v21\n"
    "v_mfma_f32_32x32x16_bf16 v[96:111], v[200:203], a[12:15], v[96:111]\n"
    "ds_read_b64_tr_b16 v[230:231], v3 offset:18208\n"
    "ds_read_b64_tr_b16 v[232:233], v3 offset:18112\n"
    "buffer_load_dwordx4 v7, s[36:39], s75 offen offset:3120 lds\n"
    "v_mov_b32 v62, s8\n"
    "v_mov_b32 v63, s9\n"
    "s_lshl_b32 s16, s97, 16\n"
    "v_mfma_f32_32x32x16_bf16 v[112:127], v[204:207], a[12:15], v[112:127]\n"
    "ds_read_b64_tr_b16 v[234:235], v3 offset:18240\n"
    "ds_read_b64_tr_b16 v[236:237], v3 offset:18144\n"
    "ds_read_b64_tr_b16 v[238:239], v3 offset:18272\n"
    "s_and_b32 s18, s97, 0xffff0000\n"
    "s_waitcnt lgkmcnt(9)\n"
    "v_mfma_f32_32x32x16_bf16 v[64:79], v[208:211], a[16:19], v[64:79]\n"
    "ds_read_b64_tr_b16 v[192:193], v3 offset:19712\n"
    "ds_read_b64_tr_b16 v[194:195], v3 offset:19840\n"
    "s_add_u32 m0, s89, 0\n"
    "s_nop 0\n"
    "buffer_load_dwordx4 v8, s[40:43], s83 offen lds\n"
    "s_cmp_lt_u32 s12, 2\n"
    "s_cselect_b32 s16, s16, s18\n"
    "v_mfma_f32_32x32x16_bf16 v[80:95], v[212:215], a[16:19], v[80:95]\n"
    "ds_read_b128 a[24:27], v2 offset:16704\n"
    "ds_read_b64_tr_b16 v[196:197], v3 offset:19744\n"
    "buffer_load_dwordx4 v9, s[44:47], s83 offen offset:1152 lds\n"
    "s_lshl_b32 s18, s96, 16\n"
    "v_mov_b32 v133, s16\n"
    "v_add_f32 v133, s18, v133\n"
    "v_mfma_f32_32x32x16_bf16 v[96:111], v[216:219], a[16:19], v[96:111]\n"
    "ds_read_b64_tr_b16 v[198:199], v3 offset:19872\n"
    "ds_read_b64_tr_b16 v[200:201], v3 offset:19776\n"
    "s_and_b32 s18, s96, 0xffff0000\n"
    "v_mov_b32 v134, s18\n"
    "v_cvt_pk_bf16_f32 v133, v133, v134\n"
    "v_cmp_eq_u32 vcc, 0, v1\n"
    "v_mfma_f32_32x32x16_bf16 v[112:127], v[220:223], a[16:19], v[112:127]\n"
    "ds_read_b64_tr_b16 v[202:203], v3 offset:19904\n"
    "ds_read_b64_tr_b16 v[204:205], v3 offset:19808\n"
    "ds_read_b64_tr_b16 v[206:207], v3 offset:19936\n"
    "buffer_load_dwordx4 v10, s[40:43], s83 offen offset:2176 lds\n"
    "v_cndmask_b32 v61, 0, v133, vcc\n"
    "s_waitcnt lgkmcnt(9)\n"
    "v_mfma_f32_32x32x16_bf16 v[64:79], v[224:227], a[20:23], v[64:79]\n"
    "ds_read_b64_tr_b16 v[208:209], v3 offset:20224\n"
    "ds_read_b64_tr_b16 v[210:211], v3 offset:20352\n"
    "ds_read_b128 a[28:31], v2 offset:16736\n"
    "ds_read_b64_tr_b16 v[212:213], v3 offset:20256\n"
    "ds_read_b64_tr_b16 v[214:215], v3 offset:20384\n"
    "s_lshl_b32 s19, s64, 2\n"
    "v_mfma_f32_32x32x16_bf16 v[80:95], v[228:231], a[20:23], v[80:95]\n"
    "ds_read_b64_tr_b16 v[216:217], v3 offset:20288\n"
    "ds_read_b64_tr_b16 v[218:219], v3 offset:20416\n"
    "ds_read_b64_tr_b16 v[220:221], v3 offset:20320\n"
    "ds_read_b64_tr_b16 v[222:223], v3 offset:20448\n"
    "buffer_load_dwordx4 v11, s[44:47], s83 offen offset:3328 lds\n"
    "s_add_u32 s19, s19, s12\n"
    "v_mfma_f32_32x32x16_bf16 v[96:111], v[232:235], a[20:23], v[96:111]\n"
    "s_mul_i32 s19, s19, 0x70\n"
    "s_lshl_b32 s16, s65, 3\n"
    "s_add_u32 s19, s19, s16\n"
    "s_lshr_b32 s16, s64, 3\n"
    "s_lshl_b32 s16, s16, 2\n"
    "s_add_u32 s19, s19, s16\n"
    "v_mfma_f32_32x32x16_bf16 v[112:127], v[236:239], a[20:23], v[112:127]\n"
    "s_lshl_b32 s19, s19, 10\n"
    "s_add_u32 s93, s19, 0x500000\n"
    "s_lshl_b32 s17, s64, 7\n"
    "s_lshl_b32 s18, s65, 3\n"
    "s_add_u32 s17, s17, s18\n"
    "s_waitcnt lgkmcnt(9)\n"
    "v_mfma_f32_32x32x16_bf16 v[64:79], v[192:195], a[24:27], v[64:79]\n"
    "s_waitcnt vmcnt(8) lgkmcnt(0)\n"
    "s_barrier\n"
    "s_mov_b64 exec, s[10:11]\n"
    "buffer_store_dwordx2 v[62:63], v53, s[60:63], 0 offen sc1\n"
    "s_mov_b64 exec, -1\n"
    "s_add_u32 s27, s17, 0x3000\n"
    "s_nop 0\n"
    "v_mfma_f32_32x32x16_bf16 v[80:95], v[196:199], a[24:27], v[80:95]\n"
    "ds_read_b64_tr_b16 v[224:225], v3 offset:35072\n"
    "ds_read_b64_tr_b16 v[226:227], v3 offset:35200\n"
    "ds_read_b128 a[32:35], v2 offset:33280\n"
    "s_add_u32 m0, s88, 0x4100\n"
    "s_nop 0\n"
    "buffer_load_dwordx4 v4, s[36:39], s76 offen lds\n"
    "s_and_b32 s49, s49, 0xffff\n"
    "v_mfma_f32_32x32x16_bf16 v[96:111], v[200:203], a[24:27], v[96:111]\n"
    "ds_read_b64_tr_b16 v[228:229], v3 offset:35104\n"
    "ds_read_b64_tr_b16 v[230:231], v3 offset:35232\n"
    "ds_read_b64_tr_b16 v[232:233], v3 offset:35136\n"
    "buffer_load_dwordx4 v5, s[36:39], s76 offen offset:1040 lds\n"
    "s_mov_b32 s50, 0x1c0000\n"
    "s_mov_b32 s51, 0x20000\n"
    "v_mfma_f32_32x32x16_bf16 v[112:127], v[204:207], a[24:27], v[112:127]\n"
    "ds_read_b64_tr_b16 v[234:235], v3 offset:35264\n"
    "ds_read_b64_tr_b16 v[236:237], v3 offset:35168\n"
    "ds_read_b64_tr_b16 v[238:239], v3 offset:35296\n"
    "s_and_b32 s57, s57, 0xffff\n"
    "s_mov_b32 s58, 0x200000\n"
    "v_mfma_f32_32x32x16_bf16 v[64:79], v[208:211], a[28:31], v[64:79]\n"
    "ds_read_b64_tr_b16 v[192:193], v3 offset:35584\n"
    "ds_read_b64_tr_b16 v[194:195], v3 offset:35712\n"
    "buffer_load_dwordx4 v6, s[36:39], s76 offen offset:2080 lds\n"
    "s_mov_b32 s59, 0x20000\n"
    "s_lshl_b32 s16, s14, 2\n"
    "s_lshr_b32 s17, s15, 3\n"
    "v_mfma_f32_32x32x16_bf16 v[80:95], v[212:215], a[28:31], v[80:95]\n"
    "ds_read_b128 a[36:39], v2 offset:33312\n"
    "ds_read_b64_tr_b16 v[196:197], v3 offset:35616\n"
    "s_add_u32 s22, s16, s17\n"
    "s_and_b32 s23, s15, 7\n"
    "s_lshl_b32 s17, s17, 1\n"
    "s_mov_b32 s18, s23\n"
    "v_mfma_f32_32x32x16_bf16 v[96:111], v[216:219], a[28:31], v[96:111]\n"
    "ds_read_b64_tr_b16 v[198:199], v3 offset:35744\n"
    "ds_read_b64_tr_b16 v[200:201], v3 offset:35648\n"
    "buffer_load_dwordx4 v7, s[36:39], s76 offen offset:3120 lds\n"
    "s_add_u32 s17, s17, s18\n"
    "s_and_b32 s17, s17, 7\n"
    "s_mul_i32 s17, s17, 7\n"
    "v_mfma_f32_32x32x16_bf16 v[112:127], v[220:223], a[28:31], v[112:127]\n"
    "ds_read_b64_tr_b16 v[202:203], v3 offset:35776\n"
    "ds_read_b64_tr_b16 v[204:205], v3 offset:35680\n"
    "ds_read_b64_tr_b16 v[206:207], v3 offset:35808\n"
    "s_lshr_b32 s17, s17, 1\n"
    "s_add_u32 s17, s17, s14\n"
    "s_waitcnt lgkmcnt(9)\n"
    "v_mfma_f32_32x32x16_bf16 v[64:79], v[224:227], a[32:35], v[64:79]\n"
    "ds_read_b64_tr_b16 v[208:209], v3 offset:37248\n"
    "ds_read_b64_tr_b16 v[210:211], v3 offset:37376\n"
    "s_add_u32 m0, s89, 0x4480\n"
    "s_nop 0\n"
    "buffer_load_dwordx4 v8, s[40:43], s84 offen lds\n"
    "s_sub_u32 s18, s17, 28\n"
    "s_min_u32 s17, s17, s18\n"
    "v_mfma_f32_32x32x16_bf16 v[80:95], v[228:231], a[32:35], v[80:95]\n"
    "ds_read_b128 a[40:43], v2 offset:33344\n"
    "ds_read_b64_tr_b16 v[212:213], v3 offset:37280\n"
    "buffer_load_dwordx4 v9, s[44:47], s84 offen offset:1152 lds\n"
    "v_add_u32 v133, s17, v1\n"
    "v_subrev_u32 v134, 28, v133\n"
    "v_cmp_gt_u32 vcc, 28, v133\n"
    "v_mfma_f32_32x32x16_bf16 v[96:111], v[232:235], a[32:35], v[96:111]\n"
    "ds_read_b64_tr_b16 v[214:215], v3 offset:37408\n"
    "ds_read_b64_tr_b16 v[216:217], v3 offset:37312\n"
    "v_cndmask_b32 v133, v134, v133, vcc\n"
    "v_lshlrev_b32 v133, 2, v133\n"
    "v_add_u32 v133, s12, v133\n"
    "v_lshlrev_b32 v245, 14, v133\n"
    "v_mfma_f32_32x32x16_bf16 v[112:127], v[236:239], a[32:35], v[112:127]\n"
    "ds_read_b64_tr_b16 v[218:219], v3 offset:37440\n"
    "ds_read_b64_tr_b16 v[220:221], v3 offset:37344\n"
    "ds_read_b64_tr_b16 v[222:223], v3 offset:37472\n"
    "buffer_load_dwordx4 v10, s[40:43], s84 offen offset:2176 lds\n"
    "s_mul_i32 s16, s22, 0x38000\n"
    "s_waitcnt lgkmcnt(9)\n"
    "v_mfma_f32_32x32x16_bf16 v[64:79], v[192:195], a[36:39], v[64:79]\n"
    "ds_read_b64_tr_b16 v[224:225], v3 offset:37760\n"
    "ds_read_b64_tr_b16 v[226:227], v3 offset:37888\n"
    "ds_read_b128 a[44:47], v2 offset:33376\n"
    "ds_read_b64_tr_b16 v[228:229], v3 offset:37792\n"
    "ds_read_b64_tr_b16 v[230:231], v3 offset:37920\n"
    "s_add_u32 s16, s16, 0x500000\n"
    "v_mfma_f32_32x32x16_bf16 v[80:95], v[196:199], a[36:39], v[80:95]\n"
    "ds_read_b64_tr_b16 v[232:233], v3 offset:37824\n"
    "ds_read_b64_tr_b16 v[234:235], v3 offset:37952\n"
    "ds_read_b64_tr_b16 v[236:237], v3 offset:37856\n"
    "ds_read_b64_tr_b16 v[238:239], v3 offset:37984\n"
    "buffer_load_dwordx4 v11, s[44:47], s84 offen offset:3328 lds\n"
    "v_lshlrev_b32 v133, 10, v133\n"
    "v_mfma_f32_32x32x16_bf16 v[96:111], v[200:203], a[36:39], v[96:111]\n"
    "v_add_u32 v244, s16, v133\n"
    "v_lshlrev_b32 v13, 4, v1\n"
    "v_lshlrev_b32 v17, 3, v131\n"
    "v_min_u32 v133, 13, v1\n"
    "v_lshlrev_b32 v18, 3, v133\n"
    "s_and_b32 s16, s12, 1\n"
    "v_mfma_f32_32x32x16_bf16 v[112:127], v[204:207], a[36:39], v[112:127]\n"
    "s_mul_i32 s16, s16, 0x1c000\n"
    "v_add_u32 v246, s16, v13\n"
    "s_sub_u32 s16, 0x1c000, s16\n"
    "v_add_u32 v247, s16, v13\n"
    "v_lshrrev_b32 v133, 4, v1\n"
    "s_waitcnt lgkmcnt(9)\n"
    "v_mfma_f32_32x32x16_bf16 v[64:79], v[208:211], a[40:43], v[64:79]\n"
    "s_waitcnt vmcnt(8) lgkmcnt(0)\n"
    "s_barrier\n"
    "v_bfe_u32 v134, v1, 3, 1\n"
    "v_lshl_add_u32 v133, v133, 2, v134\n"
    "v_and_b32 v134, 7, v1\n"
    "v_lshlrev_b32 v134, 4, v134\n"
    "v_mfma_f32_32x32x16_bf16 v[80:95], v[212:215], a[40:43], v[80:95]\n"
    "ds_read_b64_tr_b16 v[192:193], v3\n"
    "ds_read_b64_tr_b16 v[194:195], v3 offset:128\n"
    "ds_read_b128 a[48:51], v2\n"
    "s_add_u32 m0, s88, 0x8200\n"
    "s_nop 0\n"
    "buffer_load_dwordx4 v4, s[36:39], s77 offen lds\n"
    "v_lshl_add_u32 v133, v133, 10, v134\n"
    "v_mfma_f32_32x32x16_bf16 v[96:111], v[216:219], a[40:43], v[96:111]\n"
    "ds_read_b64_tr_b16 v[196:197], v3 offset:32\n"
    "ds_read_b64_tr_b16 v[198:199], v3 offset:160\n"
    "ds_read_b64_tr_b16 v[200:201], v3 offset:64\n"
    "buffer_load_dwordx4 v5, s[36:39], s77 offen offset:1040 lds\n"
    "s_lshl_b32 s16, s23, 7\n"
    "v_add_u32 v242, s16, v133\n"
    "v_mfma_f32_32x32x16_bf16 v[112:127], v[220:223], a[40:43], v[112:127]\n"
    "ds_read_b64_tr_b16 v[202:203], v3 offset:192\n"
    "ds_read_b64_tr_b16 v[204:205], v3 offset:96\n"
    "ds_read_b64_tr_b16 v[206:207], v3 offset:224\n"
    "v_add_u32 v243, 0x3c0, v242\n"
    "s_mul_i32 s90, s12, 0x2a80\n"
    "v_mfma_f32_32x32x16_bf16 v[64:79], v[224:227], a[44:47], v[64:79]\n"
    "ds_read_b64_tr_b16 v[208:209], v3 offset:512\n"
    "ds_read_b64_tr_b16 v[210:211], v3 offset:640\n"
    "buffer_load_dwordx4 v6, s[36:39], s77 offen offset:2080 lds\n"
    "s_add_u32 s91, s90, 0x1d4c0\n"
    "s_mul_i32 s92, s12, 0x2200\n"
    "s_add_u32 s92, s92, 0xc300\n"
    "v_mfma_f32_32x32x16_bf16 v[80:95], v[228:231], a[44:47], v[80:95]\n"
    "ds_read_b128 a[52:55], v2 offset:32\n"
    "ds_read_b64_tr_b16 v[212:213], v3 offset:544\n"
    "v_lshrrev_b32 v133, 1, v130\n"
    "v_mul_u32_u24 v133, 0x440, v133\n"
    "v_lshl_add_u32 v133, v129, 8, v133\n"
    "v_and_b32 v134, 1, v130\n"
    "v_mfma_f32_32x32x16_bf16 v[96:111], v[232:235], a[44:47], v[96:111]\n"
    "ds_read_b64_tr_b16 v[214:215], v3 offset:672\n"
    "ds_read_b64_tr_b16 v[216:217], v3 offset:576\n"
    "buffer_load_dwordx4 v7, s[36:39], s77 offen offset:3120 lds\n"
    "v_lshl_add_u32 v133, v134, 7, v133\n"
    "v_lshl_add_u32 v133, v132, 5, v133\n"
    "v_lshl_add_u32 v133, v131, 3, v133\n"
    "v_mfma_f32_32x32x16_bf16 v[112:127], v[236:239], a[44:47], v[112:127]\n"
    "ds_read_b64_tr_b16 v[218:219], v3 offset:704\n"
    "ds_read_b64_tr_b16 v[220:221], v3 offset:608\n"
    "ds_read_b64_tr_b16 v[222:223], v3 offset:736\n"
    "s_lshr_b32 s16, s12, 1\n"
    "s_lshl_b32 s16, s16, 6\n"
    "s_waitcnt lgkmcnt(9)\n"
    "v_mfma_f32_32x32x16_bf16 v[64:79], v[192:195], a[48:51], v[64:79]\n"
    "ds_read_b64_tr_b16 v[224:225], v3 offset:2176\n"
    "ds_read_b64_tr_b16 v[226:227], v3 offset:2304\n"
    "s_add_u32 m0, s89, 0x8900\n"
    "s_nop 0\n"
    "buffer_load_dwordx4 v8, s[40:43], s85 offen lds\n"
    "s_xor_b32 s18, s16, 64\n"
    "v_add_u32 v134, s18, v133\n"
    "v_mfma_f32_32x32x16_bf16 v[80:95], v[196:199], a[48:51], v[80:95]\n"
    "ds_read_b128 a[56:59], v2 offset:64\n"
    "ds_read_b64_tr_b16 v[228:229], v3 offset:2208\n"
    "buffer_load_dwordx4 v9, s[44:47], s85 offen offset:1152 lds\n"
    "v_add_u32 v133, s16, v133\n"
    "v_add_u32 v248, s90, v133\n"
    "v_add_u32 v249, s90, v134\n"
    "v_mfma_f32_32x32x16_bf16 v[96:111], v[200:203], a[48:51], v[96:111]\n"
    "ds_read_b64_tr_b16 v[230:231], v3 offset:2336\n"
    "ds_read_b64_tr_b16 v[232:233], v3 offset:2240\n"
    "v_add_u32 v250, s91, v133\n"
    "v_add_u32 v251, s91, v134\n"
    "v_add_u32 v252, s92, v133\n"
    "v_add_u32 v253, s92, v134\n"
    "v_mfma_f32_32x32x16_bf16 v[112:127], v[204:207], a[48:51], v[112:127]\n"
    "ds_read_b64_tr_b16 v[234:235], v3 offset:2368\n"
    "ds_read_b64_tr_b16 v[236:237], v3 offset:2272\n"
    "ds_read_b64_tr_b16 v[238:239], v3 offset:2400\n"
    "buffer_load_dwordx4 v10, s[40:43], s85 offen offset:2176 lds\n"
    "s_lshr_b32 s16, s13, 5\n"
    "s_waitcnt lgkmcnt(9)\n"
    "v_mfma_f32_32x32x16_bf16 v[64:79], v[208:211], a[52:55], v[64:79]\n"
    "ds_read_b64_tr_b16 v[192:193], v3 offset:2688\n"
    "ds_read_b64_tr_b16 v[194:195], v3 offset:2816\n"
    "ds_read_b128 a[60:63], v2 offset:96\n"
    "ds_read_b64_tr_b16 v[196:197], v3 offset:2720\n"
    "ds_read_b64_tr_b16 v[198:199], v3 offset:2848\n"
    "s_lshl_b32 s18, s16, 7\n"
    "v_mfma_f32_32x32x16_bf16 v[80:95], v[212:215], a[52:55], v[80:95]\n"
    "ds_read_b64_tr_b16 v[200:201], v3 offset:2752\n"
    "ds_read_b64_tr_b16 v[202:203], v3 offset:2880\n"
    "ds_read_b64_tr_b16 v[204:205], v3 offset:2784\n"
    "ds_read_b64_tr_b16 v[206:207], v3 offset:2912\n"
    "buffer_load_dwordx4 v11, s[44:47], s85 offen offset:3328 lds\n"
    "s_add_u32 s24, s18, 0x4000\n"
    "v_mfma_f32_32x32x16_bf16 v[96:111], v[216:219], a[52:55], v[96:111]\n"
    "s_lshl_b32 s18, s13, 3\n"
    "s_add_u32 s28, s18, 0x5000\n"
    "s_lshr_b32 s18, s22, 1\n"
    "s_lshl_b32 s16, s18, 14\n"
    "s_add_u32 s68, s16, 0x10000\n"
    "s_lshl_b32 s18, s18, 7\n"
    "v_mfma_f32_32x32x16_bf16 v[112:127], v[220:223], a[52:55], v[112:127]\n"
    "s_add_u32 s25, s18, 0x3000\n"
    "s_lshl_b32 s18, s23, 8\n"
    "s_add_u32 s26, s18, 0x5000\n"
    "s_lshl_b32 s18, s13, 13\n"
    "s_lshl_b32 s16, s12, 11\n"
    "s_waitcnt lgkmcnt(9)\n"
    "v_mfma_f32_32x32x16_bf16 v[64:79], v[224:227], a[56:59], v[64:79]\n"
    "s_waitcnt vmcnt(8) lgkmcnt(0)\n"
    "s_barrier\n"
    "s_add_u32 s31, s18, s16\n"
    "s_add_u32 s29, s31, 0x100000\n"
    "s_add_u32 s30, s31, 0x300000\n"
    "v_mfma_f32_32x32x16_bf16 v[80:95], v[228:231], a[56:59], v[80:95]\n"
    "ds_read_b64_tr_b16 v[208:209], v3 offset:17536\n"
    "ds_read_b64_tr_b16 v[210:211], v3 offset:17664\n"
    "ds_read_b128 a[64:67], v2 offset:16640\n"
    "s_add_u32 m0, s88, 0\n"
    "s_nop 0\n"
    "buffer_load_dwordx4 v4, s[36:39], s78 offen lds\n"
    "v_mfma_f32_32x32x16_bf16 v[96:111], v[232:235], a[56:59], v[96:111]\n"
    "ds_read_b64_tr_b16 v[212:213], v3 offset:17568\n"
    "ds_read_b64_tr_b16 v[214:215], v3 offset:17696\n"
    "ds_read_b64_tr_b16 v[216:217], v3 offset:17600\n"
    "buffer_load_dwordx4 v5, s[36:39], s78 offen offset:1040 lds\n"
    "s_cmp_eq_u32 s12, 0\n"
    "s_cselect_b64 s[10:11], 3, 0\n"
    "v_mfma_f32_32x32x16_bf16 v[112:127], v[236:239], a[56:59], v[112:127]\n"
    "ds_read_b64_tr_b16 v[218:219], v3 offset:17728\n"
    "ds_read_b64_tr_b16 v[220:221], v3 offset:17632\n"
    "ds_read_b64_tr_b16 v[222:223], v3 offset:17760\n"
    "s_and_b32 s19, s22, 1\n"
    "s_lshl_b32 s19, s19, 9\n"
    "v_mfma_f32_32x32x16_bf16 v[64:79], v[192:195], a[60:63], v[64:79]\n"
    "ds_read_b64_tr_b16 v[224:225], v3 offset:18048\n"
    "ds_read_b64_tr_b16 v[226:227], v3 offset:18176\n"
    "buffer_load_dwordx4 v6, s[36:39], s78 offen offset:2080 lds\n"
    "s_and_b32 s16, s12, 1\n"
    "s_lshl_b32 s16, s16, 8\n"
    "s_add_u32 s19, s19, s16\n"
    "v_mfma_f32_32x32x16_bf16 v[80:95], v[196:199], a[60:63], v[80:95]\n"
    "ds_read_b128 a[68:71], v2 offset:16672\n"
    "ds_read_b64_tr_b16 v[228:229], v3 offset:18080\n"
    "v_lshlrev_b32 v133, 3, v128\n"
    "v_lshl_add_u32 v133, v129, 12, v133\n"
    "v_add_u32 v35, s19, v133\n"
    "s_lshr_b32 s16, s12, 1\n"
    "v_mfma_f32_32x32x16_bf16 v[96:111], v[200:203], a[60:63], v[96:111]\n"
    "ds_read_b64_tr_b16 v[230:231], v3 offset:18208\n"
    "ds_read_b64_tr_b16 v[232:233], v3 offset:18112\n"
    "buffer_load_dwordx4 v7, s[36:39], s78 offen offset:3120 lds\n"
    "s_mul_i32 s16, s16, 0x1020\n"
    "s_add_u32 s16, s16, 0x19080\n"
    "s_and_b32 s18, s12, 1\n"
    "v_mfma_f32_32x32x16_bf16 v[112:127], v[204:207], a[60:63], v[112:127]\n"
    "ds_read_b64_tr_b16 v[234:235], v3 offset:18240\n"
    "ds_read_b64_tr_b16 v[236:237], v3 offset:18144\n"
    "ds_read_b64_tr_b16 v[238:239], v3 offset:18272\n"
    "s_lshl_b32 s18, s18, 11\n"
    "s_add_u32 s69, s16, s18\n"
    "s_waitcnt lgkmcnt(9)\n"
    "v_mfma_f32_32x32x16_bf16 v[64:79], v[208:211], a[64:67], v[64:79]\n"
    "ds_read_b64_tr_b16 v[192:193], v3 offset:19712\n"
    "ds_read_b64_tr_b16 v[194:195], v3 offset:19840\n"
    "s_add_u32 m0, s89, 0\n"
    "s_nop 0\n"
    "buffer_load_dwordx4 v8, s[40:43], s86 offen lds\n"
    "s_mul_i32 s18, s12, 0x900\n"
    "s_add_u32 s18, s18, 0x1b0c0\n"
    "v_mfma_f32_32x32x16_bf16 v[80:95], v[212:215], a[64:67], v[80:95]\n"
    "ds_read_b128 a[72:75], v2 offset:16704\n"
    "ds_read_b64_tr_b16 v[196:197], v3 offset:19744\n"
    "buffer_load_dwordx4 v9, s[44:47], s86 offen offset:1152 lds\n"
    "v_lshrrev_b32 v134, 4, v1\n"
    "v_and_b32 v135, 15, v1\n"
    "v_add_u32 v133, v130, v131\n"
    "v_mfma_f32_32x32x16_bf16 v[96:111], v[216:219], a[64:67], v[96:111]\n"
    "ds_read_b64_tr_b16 v[198:199], v3 offset:19872\n"
    "ds_read_b64_tr_b16 v[200:201], v3 offset:19776\n"
    "v_lshlrev_b32 v133, 3, v133\n"
    "v_lshl_add_u32 v133, v134, 6, v133\n"
    "v_add_u32 v133, 8, v133\n"
    "v_add_u32 v56, s16, v133\n"
    "v_mfma_f32_32x32x16_bf16 v[112:127], v[220:223], a[64:67], v[112:127]\n"
    "ds_read_b64_tr_b16 v[202:203], v3 offset:19904\n"
    "ds_read_b64_tr_b16 v[204:205], v3 offset:19808\n"
    "ds_read_b64_tr_b16 v[206:207], v3 offset:19936\n"
    "buffer_load_dwordx4 v10, s[40:43], s86 offen offset:2176 lds\n"
    "v_lshlrev_b32 v133, 3, v135\n"
    "s_waitcnt lgkmcnt(9)\n"
    "v_mfma_f32_32x32x16_bf16 v[64:79], v[224:227], a[68:71], v[64:79]\n"
    "ds_read_b64_tr_b16 v[208:209], v3 offset:20224\n"
    "ds_read_b64_tr_b16 v[210:211], v3 offset:20352\n"
    "ds_read_b128 a[76:79], v2 offset:16736\n"
    "ds_read_b64_tr_b16 v[212:213], v3 offset:20256\n"
    "ds_read_b64_tr_b16 v[214:215], v3 offset:20384\n"
    "v_sub_u32 v133, 0x78, v133\n"
    "v_mfma_f32_32x32x16_bf16 v[80:95], v[228:231], a[68:71], v[80:95]\n"
    "ds_read_b64_tr_b16 v[216:217], v3 offset:20288\n"
    "ds_read_b64_tr_b16 v[218:219], v3 offset:20416\n"
    "ds_read_b64_tr_b16 v[220:221], v3 offset:20320\n"
    "ds_read_b64_tr_b16 v[222:223], v3 offset:20448\n"
    "buffer_load_dwordx4 v11, s[44:47], s86 offen offset:3328 lds\n"
    "v_lshl_add_u32 v133, v134, 4, v133\n"
    "v_mfma_f32_32x32x16_bf16 v[96:111], v[232:235], a[68:71], v[96:111]\n"
    "v_add_u32 v57, s18, v133\n"
    "v_add_u32 v58, s18, v13\n"
    "v_mul_u32_u24 v133, 0x480, v129\n"
    "v_lshl_add_u32 v133, v128, 2, v133\n"
    "v_add_u32 v133, 0x400, v133\n"
    "v_add_u32 v59, s18, v133\n"
    "v_mfma_f32_32x32x16_bf16 v[112:127], v[236:239], a[68:71], v[112:127]\n"
    "v_and_b32 v133, 7, v1\n"
    "v_lshlrev_b32 v133, 2, v133\n"
    "v_cmp_eq_u32 vcc, 0, v1\n"
    "s_nop 1\n"
    "v_cndmask_b32 v133, v133, 32, vcc\n"
    "v_add_u32 v60, s16, v133\n"
    "s_waitcnt lgkmcnt(9)\n"
    "v_mfma_f32_32x32x16_bf16 v[64:79], v[192:195], a[72:75], v[64:79]\n"
    "s_waitcnt vmcnt(8) lgkmcnt(0)\n"
    "s_barrier\n"
    "v_add_u32 v133, 47, v1\n"
    "v_cndmask_b32 v133, v133, 63, vcc\n"
    "v_subrev_u32 v50, 16, v1\n"
    "v_cmp_gt_u32 vcc, 16, v1\n"
    "v_mfma_f32_32x32x16_bf16 v[80:95], v[196:199], a[72:75], v[80:95]\n"
    "ds_read_b64_tr_b16 v[224:225], v3 offset:35072\n"
    "ds_read_b64_tr_b16 v[226:227], v3 offset:35200\n"
    "ds_read_b128 a[80:83], v2 offset:33280\n"
    "s_add_u32 m0, s88, 0x4100\n"
    "s_nop 0\n"
    "buffer_load_dwordx4 v4, s[36:39], s79 offen lds\n"
    "v_cndmask_b32 v50, v50, v133, vcc\n"
    "v_mfma_f32_32x32x16_bf16 v[96:111], v[200:203], a[72:75], v[96:111]\n"
    "ds_read_b64_tr_b16 v[228:229], v3 offset:35104\n"
    "ds_read_b64_tr_b16 v[230:231], v3 offset:35232\n"
    "ds_read_b64_tr_b16 v[232:233], v3 offset:35136\n"
    "buffer_load_dwordx4 v5, s[36:39], s79 offen offset:1040 lds\n"
    "v_lshlrev_b32 v50, 2, v50\n"
    "s_mul_i32 s16, s12, 0x2020\n"
    "v_mfma_f32_32x32x16_bf16 v[112:127], v[204:207], a[72:75], v[112:127]\n"
    "ds_read_b64_tr_b16 v[234:235], v3 offset:35264\n"
    "ds_read_b64_tr_b16 v[236:237], v3 offset:35168\n"
    "ds_read_b64_tr_b16 v[238:239], v3 offset:35296\n"
    "s_add_u32 s16, s16, 0x14b00\n"
    "s_and_b32 s18, s12, 1\n"
    "v_mfma_f32_32x32x16_bf16 v[64:79], v[208:211], a[76:79], v[64:79]\n"
    "ds_read_b64_tr_b16 v[192:193], v3 offset:35584\n"
    "ds_read_b64_tr_b16 v[194:195], v3 offset:35712\n"
    "buffer_load_dwordx4 v6, s[36:39], s79 offen offset:2080 lds\n"
    "v_lshlrev_b32 v134, 1, v134\n"
    "v_lshl_add_u32 v133, v135, 3, v134\n"
    "v_add_u32 v133, s18, v133\n"
    "v_mfma_f32_32x32x16_bf16 v[80:95], v[212:215], a[76:79], v[80:95]\n"
    "ds_read_b128 a[84:87], v2 offset:33312\n"
    "ds_read_b64_tr_b16 v[196:197], v3 offset:35616\n"
    "v_bfe_u32 v134, v133, 4, 3\n"
    "v_xor_b32 v134, v133, v134\n"
    "v_lshlrev_b32 v134, 3, v134\n"
    "v_add_u32 v42, s16, v134\n"
    "v_mfma_f32_32x32x16_bf16 v[96:111], v[216:219], a[76:79], v[96:111]\n"
    "ds_read_b64_tr_b16 v[198:199], v3 offset:35744\n"
    "ds_read_b64_tr_b16 v[200:201], v3 offset:35648\n"
    "buffer_load_dwordx4 v7, s[36:39], s79 offen offset:3120 lds\n"
    "v_add_u32 v133, 1, v133\n"
    "v_bfe_u32 v134, v133, 4, 3\n"
    "v_xor_b32 v134, v133, v134\n"
    "v_mfma_f32_32x32x16_bf16 v[112:127], v[220:223], a[76:79], v[112:127]\n"
    "ds_read_b64_tr_b16 v[202:203], v3 offset:35776\n"
    "ds_read_b64_tr_b16 v[204:205], v3 offset:35680\n"
    "ds_read_b64_tr_b16 v[206:207], v3 offset:35808\n"
    "v_lshlrev_b32 v134, 3, v134\n"
    "v_add_u32 v43, s16, v134\n"
    "s_waitcnt lgkmcnt(9)\n"
    "v_mfma_f32_32x32x16_bf16 v[64:79], v[224:227], a[80:83], v[64:79]\n"
    "ds_read_b64_tr_b16 v[208:209], v3 offset:37248\n"
    "ds_read_b64_tr_b16 v[210:211], v3 offset:37376\n"
    "s_add_u32 m0, s89, 0x4480\n"
    "s_nop 0\n"
    "buffer_load_dwordx4 v8, s[40:43], s87 offen lds\n"
    "v_add_u32 v133, 1, v133\n"
    "v_mfma_f32_32x32x16_bf16 v[80:95], v[228:231], a[80:83], v[80:95]\n"
    "ds_read_b128 a[88:91], v2 offset:33344\n"
    "ds_read_b64_tr_b16 v[212:213], v3 offset:37280\n"
    "buffer_load_dwordx4 v9, s[44:47], s87 offen offset:1152 lds\n"
    "s_cmp_eq_u32 s18, 0\n"
    "s_cselect_b32 s19, 0x2010, 0\n"
    "s_add_u32 s19, s19, s16\n"
    "v_mfma_f32_32x32x16_bf16 v[96:111], v[232:235], a[80:83], v[96:111]\n"
    "ds_read_b64_tr_b16 v[214:215], v3 offset:37408\n"
    "ds_read_b64_tr_b16 v[216:217], v3 offset:37312\n"
    "v_and_b32 v133, 1, v1\n"
    "v_lshlrev_b32 v133, 2, v133\n"
    "v_add_u32 v51, s19, v133\n"
    "s_lshr_b32 s16, s12, 1\n"
    "v_mfma_f32_32x32x16_bf16 v[112:127], v[236:239], a[80:83], v[112:127]\n"
    "ds_read_b64_tr_b16 v[218:219], v3 offset:37440\n"
    "ds_read_b64_tr_b16 v[220:221], v3 offset:37344\n"
    "ds_read_b64_tr_b16 v[222:223], v3 offset:37472\n"
    "buffer_load_dwordx4 v10, s[40:43], s87 offen offset:2176 lds\n"
    "s_mul_i32 s16, s16, 0x4040\n"
    "s_waitcnt lgkmcnt(9)\n"
    "v_mfma_f32_32x32x16_bf16 v[64:79], v[192:195], a[84:87], v[64:79]\n"
    "ds_read_b64_tr_b16 v[224:225], v3 offset:37760\n"
    "ds_read_b64_tr_b16 v[226:227], v3 offset:37888\n"
    "ds_read_b128 a[92:95], v2 offset:33376\n"
    "ds_read_b64_tr_b16 v[228:229], v3 offset:37792\n"
    "ds_read_b64_tr_b16 v[230:231], v3 offset:37920\n"
    "s_lshl_b32 s18, s18, 12\n"
    "v_mfma_f32_32x32x16_bf16 v[80:95], v[196:199], a[84:87], v[80:95]\n"
    "ds_read_b64_tr_b16 v[232:233], v3 offset:37824\n"
    "ds_read_b64_tr_b16 v[234:235], v3 offset:37952\n"
    "ds_read_b64_tr_b16 v[236:237], v3 offset:37856\n"
    "ds_read_b64_tr_b16 v[238:239], v3 offset:37984\n"
    "buffer_load_dwordx4 v11, s[44:47], s87 offen offset:3328 lds\n"
    "s_add_u32 s16, s16, s18\n"
    "v_mfma_f32_32x32x16_bf16 v[96:111], v[200:203], a[84:87], v[96:111]\n"
    "s_add_u32 s16, s16, 0x14b00\n"
    "v_bfe_u32 v134, v1, 2, 3\n"
    "v_lshlrev_b32 v133, 2, v1\n"
    "v_xor_b32 v133, v133, v134\n"
    "v_lshlrev_b32 v133, 3, v133\n"
    "v_add_u32 v38, s16, v133\n"
    "v_mfma_f32_32x32x16_bf16 v[112:127], v[204:207], a[84:87], v[112:127]\n"
    "v_xor_b32 v134, 8, v133\n"
    "v_add_u32 v39, s16, v134\n"
    "v_xor_b32 v134, 16, v133\n"
    "v_add_u32 v40, s16, v134\n"
    "v_xor_b32 v134, 24, v133\n"
    "v_add_u32 v41, s16, v134\n"
    "s_waitcnt lgkmcnt(9)\n"
    "v_mfma_f32_32x32x16_bf16 v[64:79], v[208:211], a[88:91], v[64:79]\n"
    "s_waitcnt vmcnt(8) lgkmcnt(0)\n"
    "s_barrier\n"
    "v_mfma_f32_32x32x16_bf16 v[80:95], v[212:215], a[88:91], v[80:95]\n"
    "ds_read_b64_tr_b16 v[192:193], v3\n"
    "ds_read_b64_tr_b16 v[194:195], v3 offset:128\n"
    "ds_read_b128 a[96:99], v2\n"
    "buffer_load_dwordx2 v[254:255], v52, s[60:63], 0 offen sc1\n"
    "v_mfma_f32_32x32x16_bf16 v[96:111], v[216:219], a[88:91], v[96:111]\n"
    "ds_read_b64_tr_b16 v[196:197], v3 offset:32\n"
    "ds_read_b64_tr_b16 v[198:199], v3 offset:160\n"
    "ds_read_b64_tr_b16 v[200:201], v3 offset:64\n"
    "v_mfma_f32_32x32x16_bf16 v[112:127], v[220:223], a[88:91], v[112:127]\n"
    "ds_read_b64_tr_b16 v[202:203], v3 offset:192\n"
    "ds_read_b64_tr_b16 v[204:205], v3 offset:96\n"
    "ds_read_b64_tr_b16 v[206:207], v3 offset:224\n"
    "s_xor_b32 s80, s80, 0x80\n"
    "s_add_u32 m0, s89, 0x8900\n"
    "v_mfma_f32_32x32x16_bf16 v[64:79], v[224:227], a[92:95], v[64:79]\n"
    "ds_read_b64_tr_b16 v[208:209], v3 offset:512\n"
    "ds_read_b64_tr_b16 v[210:211], v3 offset:640\n"
    "buffer_load_dwordx4 v8, s[40:43], s80 offen lds\n"
    "v_mfma_f32_32x32x16_bf16 v[80:95], v[228:231], a[92:95], v[80:95]\n"
    "ds_read_b128 a[100:103], v2 offset:32\n"
    "ds_read_b64_tr_b16 v[212:213], v3 offset:544\n"
    "v_mfma_f32_32x32x16_bf16 v[96:111], v[232:235], a[92:95], v[96:111]\n"
    "ds_read_b64_tr_b16 v[214:215], v3 offset:672\n"
    "ds_read_b64_tr_b16 v[216:217], v3 offset:576\n"
    "buffer_load_dwordx4 v9, s[44:47], s80 offen offset:1152 lds\n"
    "v_mfma_f32_32x32x16_bf16 v[112:127], v[236:239], a[92:95], v[112:127]\n"
    "ds_read_b64_tr_b16 v[218:219], v3 offset:704\n"
    "ds_read_b64_tr_b16 v[220:221], v3 offset:608\n"
    "ds_read_b64_tr_b16 v[222:223], v3 offset:736\n"
    "s_waitcnt lgkmcnt(9)\n"
    "v_mfma_f32_32x32x16_bf16 v[64:79], v[192:195], a[96:99], v[64:79]\n"
    "ds_read_b64_tr_b16 v[224:225], v3 offset:2176\n"
    "ds_read_b64_tr_b16 v[226:227], v3 offset:2304\n"
    "v_mfma_f32_32x32x16_bf16 v[80:95], v[196:199], a[96:99], v[80:95]\n"
    "ds_read_b128 a[104:107], v2 offset:64\n"
    "ds_read_b64_tr_b16 v[228:229], v3 offset:2208\n"
    "buffer_load_dwordx4 v10, s[40:43], s80 offen offset:2176 lds\n"
    "v_mfma_f32_32x32x16_bf16 v[96:111], v[200:203], a[96:99], v[96:111]\n"
    "ds_read_b64_tr_b16 v[230:231], v3 offset:2336\n"
    "ds_read_b64_tr_b16 v[232:233], v3 offset:2240\n"
    "v_mfma_f32_32x32x16_bf16 v[112:127], v[204:207], a[96:99], v[112:127]\n"
    "ds_read_b64_tr_b16 v[234:235], v3 offset:2368\n"
    "ds_read_b64_tr_b16 v[236:237], v3 offset:2272\n"
    "ds_read_b64_tr_b16 v[238:239], v3 offset:2400\n"
    "s_waitcnt lgkmcnt(9)\n"
    "v_mfma_f32_32x32x16_bf16 v[64:79], v[208:211], a[100:103], v[64:79]\n"
    "ds_read_b64_tr_b16 v[192:193], v3 offset:2688\n"
    "ds_read_b64_tr_b16 v[194:195], v3 offset:2816\n"
    "ds_read_b128 a[108:111], v2 offset:96\n"
    "ds_read_b64_tr_b16 v[196:197], v3 offset:2720\n"
    "ds_read_b64_tr_b16 v[198:199], v3 offset:2848\n"
    "buffer_load_dwordx4 v11, s[44:47], s80 offen offset:3328 lds\n"
    "v_mfma_f32_32x32x16_bf16 v[80:95], v[212:215], a[100:103], v[80:95]\n"
    "ds_read_b64_tr_b16 v[200:201], v3 offset:2752\n"
    "ds_read_b64_tr_b16 v[202:203], v3 offset:2880\n"
    "ds_read_b64_tr_b16 v[204:205], v3 offset:2784\n"
    "ds_read_b64_tr_b16 v[206:207], v3 offset:2912\n"
    "v_mfma_f32_32x32x16_bf16 v[96:111], v[216:219], a[100:103], v[96:111]\n"
    "v_mfma_f32_32x32x16_bf16 v[112:127], v[220:223], a[100:103], v[112:127]\n"
    "s_waitcnt lgkmcnt(9)\n"
    "v_mfma_f32_32x32x16_bf16 v[64:79], v[224:227], a[104:107], v[64:79]\n"
    "s_waitcnt vmcnt(4) lgkmcnt(0)\n"
    "s_barrier\n"
    "v_cmp_ne_u64 vcc, s[8:9], v[254:255]\n"
    "s_nop 1\n"
    "s_cbranch_vccnz .Lspin0_%=\n"
    ".Lret0_%=:\n"
    "v_mfma_f32_32x32x16_bf16 v[80:95], v[228:231], a[104:107], v[80:95]\n"
    "ds_read_b64_tr_b16 v[208:209], v3 offset:17536\n"
    "ds_read_b64_tr_b16 v[210:211], v3 offset:17664\n"
    "ds_read_b128 a[112:115], v2 offset:16640\n"
    "buffer_load_dwordx2 v[24:25], v54, s[60:63], s94 offen sc1\n"
    "buffer_load_dwordx2 v[26:27], v54, s[60:63], s94 offen offset:1024 sc1\n"
    "v_mfma_f32_32x32x16_bf16 v[96:111], v[232:235], a[104:107], v[96:111]\n"
    "ds_read_b64_tr_b16 v[212:213], v3 offset:17568\n"
    "ds_read_b64_tr_b16 v[214:215], v3 offset:17696\n"
    "ds_read_b64_tr_b16 v[216:217], v3 offset:17600\n"
    "buffer_load_dwordx2 v[28:29], v54, s[60:63], s94 offen offset:2048 sc1\n"
    "v_mfma_f32_32x32x16_bf16 v[112:127], v[236:239], a[104:107], v[112:127]\n"
    "ds_read_b64_tr_b16 v[218:219], v3 offset:17728\n"
    "ds_read_b64_tr_b16 v[220:221], v3 offset:17632\n"
    "ds_read_b64_tr_b16 v[222:223], v3 offset:17760\n"
    "buffer_load_dwordx2 v[30:31], v54, s[60:63], s94 offen offset:3072 sc1\n"
    "v_mfma_f32_32x32x16_bf16 v[64:79], v[192:195], a[108:111], v[64:79]\n"
    "ds_read_b64_tr_b16 v[224:225], v3 offset:18048\n"
    "ds_read_b64_tr_b16 v[226:227], v3 offset:18176\n"
    "buffer_load_dwordx4 a[128:131], v13, s[60:63], s95 offen sc1\n"
    "buffer_load_dwordx4 a[132:135], v13, s[60:63], s95 offen offset:1024 sc1\n"
    "v_mfma_f32_32x32x16_bf16 v[80:95], v[196:199], a[108:111], v[80:95]\n"
    "ds_read_b128 a[116:119], v2 offset:16672\n"
    "ds_read_b64_tr_b16 v[228:229], v3 offset:18080\n"
    "buffer_load_dwordx4 a[136:139], v13, s[60:63], s95 offen offset:2048 sc1\n"
    "v_mfma_f32_32x32x16_bf16 v[96:111], v[200:203], a[108:111], v[96:111]\n"
    "ds_read_b64_tr_b16 v[230:231], v3 offset:18208\n"
    "ds_read_b64_tr_b16 v[232:233], v3 offset:18112\n"
    "buffer_load_dwordx4 a[140:143], v13, s[60:63], s95 offen offset:3072 sc1\n"
    "v_mfma_f32_32x32x16_bf16 v[112:127], v[204:207], a[108:111], v[112:127]\n"
    "ds_read_b64_tr_b16 v[234:235], v3 offset:18240\n"
    "ds_read_b64_tr_b16 v[236:237], v3 offset:18144\n"
    "ds_read_b64_tr_b16 v[238:239], v3 offset:18272\n"
    "s_xor_b32 s95, s95, 0x1000\n"
    "buffer_load_dwordx4 a[144:147], v13, s[60:63], s95 offen sc1\n"
    "s_waitcnt lgkmcnt(9)\n"
    "v_mfma_f32_32x32x16_bf16 v[64:79], v[208:211], a[112:115], v[64:79]\n"
    "ds_read_b64_tr_b16 v[192:193], v3 offset:19712\n"
    "ds_read_b64_tr_b16 v[194:195], v3 offset:19840\n"
    "buffer_load_dwordx4 a[148:151], v13, s[60:63], s95 offen offset:1024 sc1\n"
    "buffer_load_dwordx4 a[152:155], v13, s[60:63], s95 offen offset:2048 sc1\n"
    "v_mfma_f32_32x32x16_bf16 v[80:95], v[212:215], a[112:115], v[80:95]\n"
    "ds_read_b128 a[120:123], v2 offset:16704\n"
    "ds_read_b64_tr_b16 v[196:197], v3 offset:19744\n"
    "buffer_load_dwordx4 a[156:159], v13, s[60:63], s95 offen offset:3072 sc1\n"
    "v_mfma_f32_32x32x16_bf16 v[96:111], v[216:219], a[112:115], v[96:111]\n"
    "ds_read_b64_tr_b16 v[198:199], v3 offset:19872\n"
    "ds_read_b64_tr_b16 v[200:201], v3 offset:19776\n"
    "s_xor_b32 s81, s81, 0x80\n"
    "s_add_u32 m0, s89, 0\n"
    "v_readlane_b32 s98, v245, 0\n"
    "v_mfma_f32_32x32x16_bf16 v[112:127], v[220:223], a[112:115], v[112:127]\n"
    "ds_read_b64_tr_b16 v[202:203], v3 offset:19904\n"
    "ds_read_b64_tr_b16 v[204:205], v3 offset:19808\n"
    "ds_read_b64_tr_b16 v[206:207], v3 offset:19936\n"
    "buffer_load_dwordx4 v8, s[40:43], s81 offen lds\n"
    "buffer_load_dwordx4 v9, s[44:47], s81 offen offset:1152 lds\n"
    "s_waitcnt lgkmcnt(9)\n"
    "v_mfma_f32_32x32x16_bf16 v[64:79], v[224:227], a[116:119], v[64:79]\n"
    "ds_read_b64_tr_b16 v[208:209], v3 offset:20224\n"
    "ds_read_b64_tr_b16 v[210:211], v3 offset:20352\n"
    "ds_read_b128 a[124:127], v2 offset:16736\n"
    "ds_read_b64_tr_b16 v[212:213], v3 offset:20256\n"
    "ds_read_b64_tr_b16 v[214:215], v3 offset:20384\n"
    "buffer_load_dwordx4 v10, s[40:43], s81 offen offset:2176 lds\n"
    "v_mfma_f32_32x32x16_bf16 v[80:95], v[228:231], a[116:119], v[80:95]\n"
    "ds_read_b64_tr_b16 v[216:217], v3 offset:20288\n"
    "ds_read_b64_tr_b16 v[218:219], v3 offset:20416\n"
    "ds_read_b64_tr_b16 v[220:221], v3 offset:20320\n"
    "ds_read_b64_tr_b16 v[222:223], v3 offset:20448\n"
    "buffer_load_dwordx4 v11, s[44:47], s81 offen offset:3328 lds\n"
    "s_add_u32 m0, s91, 0\n"
    "v_mfma_f32_32x32x16_bf16 v[96:111], v[232:235], a[116:119], v[96:111]\n"
    "buffer_load_dwordx4 v242, s[48:51], s98 offen lds\n"
    "buffer_load_dwordx4 v243, s[48:51], s98 offen offset:1088 lds\n"
    "v_mfma_f32_32x32x16_bf16 v[112:127], v[236:239], a[116:119], v[112:127]\n"
    "s_waitcnt lgkmcnt(9)\n"
    "v_mfma_f32_32x32x16_bf16 v[64:79], v[192:195], a[120:123], v[64:79]\n"
    "s_waitcnt vmcnt(18) lgkmcnt(0)\n"
    "s_barrier\n"
    "v_mfma_f32_32x32x16_bf16 v[80:95], v[196:199], a[120:123], v[80:95]\n"
    "ds_read_b64_tr_b16 v[224:225], v3 offset:35072\n"
    "ds_read_b64_tr_b16 v[226:227], v3 offset:35200\n"
    "ds_read_b64_tr_b16 v[228:229], v3 offset:35104\n"
    "s_xor_b32 s82, s82, 0x80\n"
    "s_add_u32 m0, s89, 0x4480\n"
    "v_readlane_b32 s35, v245, 1\n"
    "v_mfma_f32_32x32x16_bf16 v[96:111], v[200:203], a[120:123], v[96:111]\n"
    "ds_read_b64_tr_b16 v[230:231], v3 offset:35232\n"
    "ds_read_b64_tr_b16 v[232:233], v3 offset:35136\n"
    "ds_read_b64_tr_b16 v[234:235], v3 offset:35264\n"
    "buffer_load_dwordx4 v8, s[40:43], s82 offen lds\n"
    "v_mfma_f32_32x32x16_bf16 v[112:127], v[204:207], a[120:123], v[112:127]\n"
    "ds_read_b64_tr_b16 v[236:237], v3 offset:35168\n"
    "ds_read_b64_tr_b16 v[238:239], v3 offset:35296\n"
    "buffer_load_dwordx4 v9, s[44:47], s82 offen offset:1152 lds\n"
    "v_mfma_f32_32x32x16_bf16 v[64:79], v[208:211], a[124:127], v[64:79]\n"
    "ds_read_b64_tr_b16 v[192:193], v3 offset:35584\n"
    "ds_read_b64_tr_b16 v[194:195], v3 offset:35712\n"
    "v_mfma_f32_32x32x16_bf16 v[80:95], v[212:215], a[124:127], v[80:95]\n"
    "ds_read_b64_tr_b16 v[196:197], v3 offset:35616\n"
    "ds_read_b64_tr_b16 v[198:199], v3 offset:35744\n"
    "buffer_load_dwordx4 v10, s[40:43], s82 offen offset:2176 lds\n"
    "v_mfma_f32_32x32x16_bf16 v[96:111], v[216:219], a[124:127], v[96:111]\n"
    "ds_read_b64_tr_b16 v[200:201], v3 offset:35648\n"
    "ds_read_b64_tr_b16 v[202:203], v3 offset:35776\n"
    "v_mfma_f32_32x32x16_bf16 v[112:127], v[220:223], a[124:127], v[112:127]\n"
    "ds_read_b64_tr_b16 v[204:205], v3 offset:35680\n"
    "ds_read_b64_tr_b16 v[206:207], v3 offset:35808\n"
    "s_waitcnt lgkmcnt(8)\n"
    "v_mfma_f32_32x32x16_bf16 v[128:143], v[224:227], a[0:3], 0\n"
    "ds_read_b64_tr_b16 v[208:209], v3 offset:37248\n"
    "ds_read_b64_tr_b16 v[210:211], v3 offset:37376\n"
    "buffer_load_dwordx4 v11, s[44:47], s82 offen offset:3328 lds\n"
    "v_mfma_f32_32x32x16_bf16 v[144:159], v[228:231], a[0:3], 0\n"
    "ds_read_b64_tr_b16 v[212:213], v3 offset:37280\n"
    "ds_read_b64_tr_b16 v[214:215], v3 offset:37408\n"
    "v_mfma_f32_32x32x16_bf16 v[160:175], v[232:235], a[0:3], 0\n"
    "ds_read_b64_tr_b16 v[216:217], v3 offset:37312\n"
    "ds_read_b64_tr_b16 v[218:219], v3 offset:37440\n"
    "s_add_u32 m0, s91, 0x880\n"
    "s_nop 0\n"
    "buffer_load_dwordx4 v242, s[48:51], s35 offen lds\n"
    "v_mfma_f32_32x32x16_bf16 v[176:191], v[236:239], a[0:3], 0\n"
    "ds_read_b64_tr_b16 v[220:221], v3 offset:37344\n"
    "ds_read_b64_tr_b16 v[222:223], v3 offset:37472\n"
    "s_waitcnt lgkmcnt(8)\n"
    "v_mfma_f32_32x32x16_bf16 v[128:143], v[192:195], a[4:7], v[128:143]\n"
    "ds_read_b64_tr_b16 v[224:225], v3 offset:37760\n"
    "ds_read_b64_tr_b16 v[226:227], v3 offset:37888\n"
    "ds_read_b64_tr_b16 v[228:229], v3 offset:37792\n"
    "ds_read_b64_tr_b16 v[230:231], v3 offset:37920\n"
    "buffer_load_dwordx4 v243, s[48:51], s35 offen offset:1088 lds\n"
    "s_waitcnt vmcnt(22)\n"
    "v_add_f32 v24, v24, v26\n"
    "v_mfma_f32_32x32x16_bf16 v[144:159], v[196:199], a[4:7], v[144:159]\n"
    "ds_read_b64_tr_b16 v[232:233], v3 offset:37824\n"
    "ds_read_b64_tr_b16 v[234:235], v3 offset:37952\n"
    "ds_read_b64_tr_b16 v[236:237], v3 offset:37856\n"
    "ds_read_b64_tr_b16 v[238:239], v3 offset:37984\n"
    "v_add_f32 v25, v25, v27\n"
    "s_waitcnt vmcnt(20)\n"
    "v_add_f32 v28, v28, v30\n"
    "v_mfma_f32_32x32x16_bf16 v[160:175], v[200:203], a[4:7], v[160:175]\n"
    "v_add_f32 v29, v29, v31\n"
    "v_add_f32 v24, v24, v28\n"
    "v_add_f32 v25, v25, v29\n"
    "v_mov_b32 v26, v24\n"
    "v_mov_b32 v27, v25\n"
    "v_mov_b32 v37, 0x3727c5ac\n"
    "v_mfma_f32_32x32x16_bf16 v[176:191], v[204:207], a[4:7], v[176:191]\n"
    "v_permlane32_swap_b32 v24, v26\n"
    "v_permlane32_swap_b32 v25, v27\n"
    "v_add_f32 v24, v24, v26\n"
    "v_add_f32 v25, v25, v27\n"
    "v_mul_f32 v20, 0xbb000000, v24\n"
    "s_waitcnt lgkmcnt(8)\n"
    "v_mfma_f32_32x32x16_bf16 v[128:143], v[208:211], a[8:11], v[128:143]\n"
    "s_waitcnt vmcnt(8) lgkmcnt(0)\n"
    "s_barrier\n"
    "v_mul_f32 v25, 0x3b000000, v25\n"
    "v_fma_f32 v25, -v20, v20, v25\n"
    "v_max_f32 v25, 0, v25\n"
    "v_add_f32 v16, v37, v25\n"
    "v_mfma_f32_32x32x16_bf16 v[144:159], v[212:215], a[8:11], v[144:159]\n"
    "ds_read_b64_tr_b16 v[192:193], v3\n"
    "ds_read_b64_tr_b16 v[194:195], v3 offset:128\n"
    "ds_read_b64_tr_b16 v[196:197], v3 offset:32\n"
    "s_xor_b32 s83, s83, 0x80\n"
    "s_add_u32 m0, s89, 0x8900\n"
    "v_readlane_b32 s98, v245, 2\n"
    "v_mfma_f32_32x32x16_bf16 v[160:175], v[216:219], a[8:11], v[160:175]\n"
    "ds_read_b64_tr_b16 v[198:199], v3 offset:160\n"
    "ds_read_b64_tr_b16 v[200:201], v3 offset:64\n"
    "ds_read_b64_tr_b16 v[202:203], v3 offset:192\n"
    "buffer_load_dwordx4 v8, s[40:43], s83 offen lds\n"
    "v_cvt_pk_bf16_f32 v20, v20, v20\n"
    "v_mfma_f32_32x32x16_bf16 v[176:191], v[220:223], a[8:11], v[176:191]\n"
    "ds_read_b64_tr_b16 v[204:205], v3 offset:96\n"
    "ds_read_b64_tr_b16 v[206:207], v3 offset:224\n"
    "buffer_load_dwordx4 v9, s[44:47], s83 offen offset:1152 lds\n"
    "v_rsq_f32 v26, v16\n"
    "v_mfma_f32_32x32x16_bf16 v[128:143], v[224:227], a[12:15], v[128:143]\n"
    "ds_read_b64_tr_b16 v[208:209], v3 offset:512\n"
    "ds_read_b64_tr_b16 v[210:211], v3 offset:640\n"
    "v_mov_b32 v21, v20\n"
    "v_mul_f32 v27, v26, v26\n"
    "v_mul_f32 v12, 0xc0135761, v26\n"
    "v_mov_b32 v22, v20\n"
    "v_mfma_f32_32x32x16_bf16 v[144:159], v[228:231], a[12:15], v[144:159]\n"
    "ds_read_b64_tr_b16 v[212:213], v3 offset:544\n"
    "ds_read_b64_tr_b16 v[214:215], v3 offset:672\n"
    "buffer_load_dwordx4 v10, s[40:43], s83 offen offset:2176 lds\n"
    "v_mul_f32 v27, v27, v26\n"
    "v_mov_b32 v23, v20\n"
    "v_mul_f32 v14, 0xbdd2d3e8, v27\n"
    "v_mfma_f32_32x32x16_bf16 v[160:175], v[232:235], a[12:15], v[160:175]\n"
    "ds_read_b64_tr_b16 v[216:217], v3 offset:576\n"
    "ds_read_b64_tr_b16 v[218:219], v3 offset:704\n"
    "v_mfma_f32_32x32x16_bf16 v[176:191], v[236:239], a[12:15], v[176:191]\n"
    "ds_read_b64_tr_b16 v[220:221], v3 offset:608\n"
    "ds_read_b64_tr_b16 v[222:223], v3 offset:736\n"
    "v_mfma_f32_32x32x16_bf16 v[64:79], a[128:131], v[20:23], v[64:79]\n"
    "buffer_load_dwordx4 v11, s[44:47], s83 offen offset:3328 lds\n"
    "v_mfma_f32_32x32x16_bf16 v[80:95], a[132:135], v[20:23], v[80:95]\n"
    "v_mfma_f32_32x32x16_bf16 v[96:111], a[136:139], v[20:23], v[96:111]\n"
    "s_add_u32 m0, s91, 0x1100\n"
    "s_nop 0\n"
    "buffer_load_dwordx4 v242, s[48:51], s98 offen lds\n"
    "v_mfma_f32_32x32x16_bf16 v[112:127], a[140:143], v[20:23], v[112:127]\n"
    "s_waitcnt lgkmcnt(8)\n"
    "v_mfma_f32_32x32x16_bf16 v[128:143], v[192:195], a[16:19], v[128:143]\n"
    "ds_read_b64_tr_b16 v[224:225], v3 offset:2176\n"
    "ds_read_b64_tr_b16 v[226:227], v3 offset:2304\n"
    "buffer_load_dwordx4 v243, s[48:51], s98 offen offset:1088 lds\n"
    "v_mfma_f32_32x32x16_bf16 v[144:159], v[196:199], a[16:19], v[144:159]\n"
    "ds_read_b64_tr_b16 v[228:229], v3 offset:2208\n"
    "ds_read_b64_tr_b16 v[230:231], v3 offset:2336\n"
    "v_mfma_f32_32x32x16_bf16 v[160:175], v[200:203], a[16:19], v[160:175]\n"
    "ds_read_b64_tr_b16 v[232:233], v3 offset:2240\n"
    "ds_read_b64_tr_b16 v[234:235], v3 offset:2368\n"
    "v_mul_f32 v44, v64, v64\n"
    "v_mul_f32 v45, v65, v65\n"
    "v_mul_f32 v46, v66, v66\n"
    "v_mul_f32 v47, v67, v67\n"
    "v_mfma_f32_32x32x16_bf16 v[176:191], v[204:207], a[16:19], v[176:191]\n"
    "ds_read_b64_tr_b16 v[236:237], v3 offset:2272\n"
    "ds_read_b64_tr_b16 v[238:239], v3 offset:2400\n"
    "v_fma_f32 v44, v14, v44, v12\n"
    "v_fma_f32 v45, v14, v45, v12\n"
    "v_fma_f32 v46, v14, v46, v12\n"
    "s_waitcnt lgkmcnt(8)\n"
    "v_mfma_f32_32x32x16_bf16 v[128:143], v[208:211], a[20:23], v[128:143]\n"
    "ds_read_b64_tr_b16 v[192:193], v3 offset:2688\n"
    "ds_read_b64_tr_b16 v[194:195], v3 offset:2816\n"
    "ds_read_b64_tr_b16 v[196:197], v3 offset:2720\n"
    "ds_read_b64_tr_b16 v[198:199], v3 offset:2848\n"
    "v_fma_f32 v47, v14, v47, v12\n"
    "v_mul_f32 v44, v44, v64\n"
    "v_mfma_f32_32x32x16_bf16 v[144:159], v[212:215], a[20:23], v[144:159]\n"
    "ds_read_b64_tr_b16 v[200:201], v3 offset:2752\n"
    "ds_read_b64_tr_b16 v[202:203], v3 offset:2880\n"
    "ds_read_b64_tr_b16 v[204:205], v3 offset:2784\n"
    "ds_read_b64_tr_b16 v[206:207], v3 offset:2912\n"
    "v_mul_f32 v45, v45, v65\n"
    "v_mul_f32 v46, v46, v66\n"
    "v_mfma_f32_32x32x16_bf16 v[160:175], v[216:219], a[20:23], v[160:175]\n"
    "v_mul_f32 v47, v47, v67\n"
    "v_exp_f32 v44, v44\n"
    "v_exp_f32 v45, v45\n"
    "v_mfma_f32_32x32x16_bf16 v[176:191], v[220:223], a[20:23], v[176:191]\n"
    "v_exp_f32 v46, v46\n"
    "v_exp_f32 v47, v47\n"
    "v_mul_f32 v72, v64, v72\n"
    "s_waitcnt lgkmcnt(8)\n"
    "v_mfma_f32_32x32x16_bf16 v[128:143], v[224:227], a[24:27], v[128:143]\n"
    "s_waitcnt vmcnt(8) lgkmcnt(0)\n"
    "s_barrier\n"
    "v_mul_f32 v73, v65, v73\n"
    "v_mul_f32 v74, v66, v74\n"
    "v_mul_f32 v75, v67, v75\n"
    "v_fma_f32 v44, v44, v16, v16\n"
    "v_mfma_f32_32x32x16_bf16 v[144:159], v[228:231], a[24:27], v[144:159]\n"
    "ds_read_b64_tr_b16 v[208:209], v3 offset:17536\n"
    "ds_read_b64_tr_b16 v[210:211], v3 offset:17664\n"
    "ds_read_b64_tr_b16 v[212:213], v3 offset:17568\n"
    "s_xor_b32 s84, s84, 0x80\n"
    "s_add_u32 m0, s89, 0\n"
    "v_readlane_b32 s35, v245, 3\n"
    "v_mfma_f32_32x32x16_bf16 v[160:175], v[232:235], a[24:27], v[160:175]\n"
    "ds_read_b64_tr_b16 v[214:215], v3 offset:17696\n"
    "ds_read_b64_tr_b16 v[216:217], v3 offset:17600\n"
    "ds_read_b64_tr_b16 v[218:219], v3 offset:17728\n"
    "buffer_load_dwordx4 v8, s[40:43], s84 offen lds\n"
    "v_fma_f32 v45, v45, v16, v16\n"
    "v_fma_f32 v46, v46, v16, v16\n"
    "v_mfma_f32_32x32x16_bf16 v[176:191], v[236:239], a[24:27], v[176:191]\n"
    "ds_read_b64_tr_b16 v[220:221], v3 offset:17632\n"
    "ds_read_b64_tr_b16 v[222:223], v3 offset:17760\n"
    "buffer_load_dwordx4 v9, s[44:47], s84 offen offset:1152 lds\n"
    "v_fma_f32 v47, v47, v16, v16\n"
    "v_mfma_f32_32x32x16_bf16 v[128:143], v[192:195], a[28:31], v[128:143]\n"
    "ds_read_b64_tr_b16 v[224:225], v3 offset:18048\n"
    "ds_read_b64_tr_b16 v[226:227], v3 offset:18176\n"
    "v_rcp_f32 v44, v44\n"
    "v_rcp_f32 v45, v45\n"
    "v_mfma_f32_32x32x16_bf16 v[144:159], v[196:199], a[28:31], v[144:159]\n"
    "ds_read_b64_tr_b16 v[228:229], v3 offset:18080\n"
    "ds_read_b64_tr_b16 v[230:231], v3 offset:18208\n"
    "buffer_load_dwordx4 v10, s[40:43], s84 offen offset:2176 lds\n"
    "v_rcp_f32 v46, v46\n"
    "v_mfma_f32_32x32x16_bf16 v[160:175], v[200:203], a[28:31], v[160:175]\n"
    "ds_read_b64_tr_b16 v[232:233], v3 offset:18112\n"
    "ds_read_b64_tr_b16 v[234:235], v3 offset:18240\n"
    "v_rcp_f32 v47, v47\n"
    "v_mul_f32 v72, v72, v44\n"
    "v_mul_f32 v73, v73, v45\n"
    "v_mfma_f32_32x32x16_bf16 v[176:191], v[204:207], a[28:31], v[176:191]\n"
    "ds_read_b64_tr_b16 v[236:237], v3 offset:18144\n"
    "ds_read_b64_tr_b16 v[238:239], v3 offset:18272\n"
    "v_mul_f32 v74, v74, v46\n"
    "v_mul_f32 v75, v75, v47\n"
    "v_cvt_pk_bf16_f32 v64, v72, v73\n"
    "s_waitcnt lgkmcnt(8)\n"
    "v_mfma_f32_32x32x16_bf16 v[128:143], v[208:211], a[32:35], v[128:143]\n"
    "ds_read_b64_tr_b16 v[192:193], v3 offset:19712\n"
    "ds_read_b64_tr_b16 v[194:195], v3 offset:19840\n"
    "buffer_load_dwordx4 v11, s[44:47], s84 offen offset:3328 lds\n"
    "v_cvt_pk_bf16_f32 v65, v74, v75\n"
    "v_mul_f32 v44, v68, v68\n"
    "v_mul_f32 v45, v69, v69\n"
    "v_mfma_f32_32x32x16_bf16 v[144:159], v[212:215], a[32:35], v[144:159]\n"
    "ds_read_b64_tr_b16 v[196:197], v3 offset:19744\n"
    "ds_read_b64_tr_b16 v[198:199], v3 offset:19872\n"
    "v_mul_f32 v46, v70, v70\n"
    "v_mul_f32 v47, v71, v71\n"
    "v_fma_f32 v44, v14, v44, v12\n"
    "v_fma_f32 v45, v14, v45, v12\n"
    "v_mfma_f32_32x32x16_bf16 v[160:175], v[216:219], a[32:35], v[160:175]\n"
    "ds_read_b64_tr_b16 v[200:201], v3 offset:19776\n"
    "ds_read_b64_tr_b16 v[202:203], v3 offset:19904\n"
    "s_add_u32 m0, s91, 0x1980\n"
    "s_nop 0\n"
    "buffer_load_dwordx4 v242, s[48:51], s35 offen lds\n"
    "v_fma_f32 v46, v14, v46, v12\n"
    "v_fma_f32 v47, v14, v47, v12\n"
    "v_mfma_f32_32x32x16_bf16 v[176:191], v[220:223], a[32:35], v[176:191]\n"
    "ds_read_b64_tr_b16 v[204:205], v3 offset:19808\n"
    "ds_read_b64_tr_b16 v[206:207], v3 offset:19936\n"
    "v_mul_f32 v44, v44, v68\n"
    "v_mul_f32 v45, v45, v69\n"
    "v_mul_f32 v46, v46, v70\n"
    "s_waitcnt lgkmcnt(8)\n"
    "v_mfma_f32_32x32x16_bf16 v[128:143], v[224:227], a[36:39], v[128:143]\n"
    "ds_read_b64_tr_b16 v[208:209], v3 offset:20224\n"
    "ds_read_b64_tr_b16 v[210:211], v3 offset:20352\n"
    "ds_read_b64_tr_b16 v[212:213], v3 offset:20256\n"
    "ds_read_b64_tr_b16 v[214:215], v3 offset:20384\n"
    "buffer_load_dwordx4 v243, s[48:51], s35 offen offset:1088 lds\n"
    "v_mul_f32 v47, v47, v71\n"
    "v_mfma_f32_32x32x16_bf16 v[144:159], v[228:231], a[36:39], v[144:159]\n"
    "ds_read_b64_tr_b16 v[216:217], v3 offset:20288\n"
    "ds_read_b64_tr_b16 v[218:219], v3 offset:20416\n"
    "ds_read_b64_tr_b16 v[220:221], v3 offset:20320\n"
    "ds_read_b64_tr_b16 v[222:223], v3 offset:20448\n"
    "v_exp_f32 v44, v44\n"
    "v_mfma_f32_32x32x16_bf16 v[160:175], v[232:235], a[36:39], v[160:175]\n"
    "v_exp_f32 v45, v45\n"
    "v_exp_f32 v46, v46\n"
    "v_exp_f32 v47, v47\n"
    "v_mfma_f32_32x32x16_bf16 v[176:191], v[236:239], a[36:39], v[176:191]\n"
    "v_mul_f32 v76, v68, v76\n"
    "v_mul_f32 v77, v69, v77\n"
    "v_mul_f32 v78, v70, v78\n"
    "v_mul_f32 v79, v71, v79\n"
    "v_fma_f32 v44, v44, v16, v16\n"
    "s_waitcnt lgkmcnt(8)\n"
    "v_mfma_f32_32x32x16_bf16 v[128:143], v[192:195], a[40:43], v[128:143]\n"
    "s_waitcnt vmcnt(8) lgkmcnt(0)\n"
    "s_barrier\n"
    "v_fma_f32 v45, v45, v16, v16\n"
    "v_fma_f32 v46, v46, v16, v16\n"
    "v_fma_f32 v47, v47, v16, v16\n"
    "v_mfma_f32_32x32x16_bf16 v[144:159], v[196:199], a[40:43], v[144:159]\n"
    "ds_read_b64_tr_b16 v[224:225], v3 offset:35072\n"
    "ds_read_b64_tr_b16 v[226:227], v3 offset:35200\n"
    "ds_read_b64_tr_b16 v[228:229], v3 offset:35104\n"
    "s_xor_b32 s85, s85, 0x80\n"
    "s_add_u32 m0, s89, 0x4480\n"
    "v_readlane_b32 s98, v245, 4\n"
    "v_mfma_f32_32x32x16_bf16 v[160:175], v[200:203], a[40:43], v[160:175]\n"
    "ds_read_b64_tr_b16 v[230:231], v3 offset:35232\n"
    "ds_read_b64_tr_b16 v[232:233], v3 offset:35136\n"
    "ds_read_b64_tr_b16 v[234:235], v3 offset:35264\n"
    "buffer_load_dwordx4 v8, s[40:43], s85 offen lds\n"
    "v_rcp_f32 v44, v44\n"
    "v_mfma_f32_32x32x16_bf16 v[176:191], v[204:207], a[40:43], v[176:191]\n"
    "ds_read_b64_tr_b16 v[236:237], v3 offset:35168\n"
    "ds_read_b64_tr_b16 v[238:239], v3 offset:35296\n"
    "buffer_load_dwordx4 v9, s[44:47], s85 offen offset:1152 lds\n"
    "v_rcp_f32 v45, v45\n"
    "v_mfma_f32_32x32x16_bf16 v[128:143], v[208:211], a[44:47], v[128:143]\n"
    "ds_read_b64_tr_b16 v[192:193], v3 offset:35584\n"
    "ds_read_b64_tr_b16 v[194:195], v3 offset:35712\n"
    "v_rcp_f32 v46, v46\n"
    "v_rcp_f32 v47, v47\n"
    "v_mfma_f32_32x32x16_bf16 v[144:159], v[212:215], a[44:47], v[144:159]\n"
    "ds_read_b64_tr_b16 v[196:197], v3 offset:35616\n"
    "ds_read_b64_tr_b16 v[198:199], v3 offset:35744\n"
    "buffer_load_dwordx4 v10, s[40:43], s85 offen offset:2176 lds\n"
    "v_mul_f32 v76, v76, v44\n"
    "v_mul_f32 v77, v77, v45\n"
    "v_mul_f32 v78, v78, v46\n"
    "v_mfma_f32_32x32x16_bf16 v[160:175], v[216:219], a[44:47], v[160:175]\n"
    "ds_read_b64_tr_b16 v[200:201], v3 offset:35648\n"
    "ds_read_b64_tr_b16 v[202:203], v3 offset:35776\n"
    "v_mul_f32 v79, v79, v47\n"
    "v_cvt_pk_bf16_f32 v66, v76, v77\n"
    "v_cvt_pk_bf16_f32 v67, v78, v79\n"
    "v_mul_f32 v44, v80, v80\n"
    "v_mfma_f32_32x32x16_bf16 v[176:191], v[220:223], a[44:47], v[176:191]\n"
    "ds_read_b64_tr_b16 v[204:205], v3 offset:35680\n"
    "ds_read_b64_tr_b16 v[206:207], v3 offset:35808\n"
    "v_mul_f32 v45, v81, v81\n"
    "v_mul_f32 v46, v82, v82\n"
    "v_mul_f32 v47, v83, v83\n"
    "s_waitcnt lgkmcnt(8)\n"
    "v_mfma_f32_32x32x16_bf16 v[128:143], v[224:227], a[48:51], v[128:143]\n"
    "ds_read_b64_tr_b16 v[208:209], v3 offset:37248\n"
    "ds_read_b64_tr_b16 v[210:211], v3 offset:37376\n"
    "buffer_load_dwordx4 v11, s[44:47], s85 offen offset:3328 lds\n"
    "v_fma_f32 v44, v14, v44, v12\n"
    "v_fma_f32 v45, v14, v45, v12\n"
    "v_fma_f32 v46, v14, v46, v12\n"
    "v_mfma_f32_32x32x16_bf16 v[144:159], v[228:231], a[48:51], v[144:159]\n"
    "ds_read_b64_tr_b16 v[212:213], v3 offset:37280\n"
    "ds_read_b64_tr_b16 v[214:215], v3 offset:37408\n"
    "v_fma_f32 v47, v14, v47, v12\n"
    "v_mul_f32 v44, v44, v80\n"
    "v_mul_f32 v45, v45, v81\n"
    "v_mul_f32 v46, v46, v82\n"
    "v_mfma_f32_32x32x16_bf16 v[160:175], v[232:235], a[48:51], v[160:175]\n"
    "ds_read_b64_tr_b16 v[216:217], v3 offset:37312\n"
    "ds_read_b64_tr_b16 v[218:219], v3 offset:37440\n"
    "s_add_u32 m0, s91, 0x2200\n"
    "s_nop 0\n"
    "buffer_load_dwordx4 v242, s[48:51], s98 offen lds\n"
    "v_mul_f32 v47, v47, v83\n"
    "v_mfma_f32_32x32x16_bf16 v[176:191], v[236:239], a[48:51], v[176:191]\n"
    "ds_read_b64_tr_b16 v[220:221], v3 offset:37344\n"
    "ds_read_b64_tr_b16 v[222:223], v3 offset:37472\n"
    "v_exp_f32 v44, v44\n"
    "s_waitcnt lgkmcnt(8)\n"
    "v_mfma_f32_32x32x16_bf16 v[128:143], v[192:195], a[52:55], v[128:143]\n"
    "ds_read_b64_tr_b16 v[224:225], v3 offset:37760\n"
    "ds_read_b64_tr_b16 v[226:227], v3 offset:37888\n"
    "ds_read_b64_tr_b16 v[228:229], v3 offset:37792\n"
    "ds_read_b64_tr_b16 v[230:231], v3 offset:37920\n"
    "buffer_load_dwordx4 v243, s[48:51], s98 offen offset:1088 lds\n"
    "v_mfma_f32_32x32x16_bf16 v[144:159], v[196:199], a[52:55], v[144:159]\n"
    "ds_read_b64_tr_b16 v[232:233], v3 offset:37824\n"
    "ds_read_b64_tr_b16 v[234:235], v3 offset:37952\n"
    "ds_read_b64_tr_b16 v[236:237], v3 offset:37856\n"
    "ds_read_b64_tr_b16 v[238:239], v3 offset:37984\n"
    "v_exp_f32 v45, v45\n"
    "v_mfma_f32_32x32x16_bf16 v[160:175], v[200:203], a[52:55], v[160:175]\n"
    "v_exp_f32 v46, v46\n"
    "v_exp_f32 v47, v47\n"
    "v_mul_f32 v88, v80, v88\n"
    "v_mul_f32 v89, v81, v89\n"
    "v_mfma_f32_32x32x16_bf16 v[176:191], v[204:207], a[52:55], v[176:191]\n"
    "v_mul_f32 v90, v82, v90\n"
    "v_mul_f32 v91, v83, v91\n"
    "v_fma_f32 v44, v44, v16, v16\n"
    "v_fma_f32 v45, v45, v16, v16\n"
    "v_fma_f32 v46, v46, v16, v16\n"
    "s_waitcnt lgkmcnt(8)\n"
    "v_mfma_f32_32x32x16_bf16 v[128:143], v[208:211], a[56:59], v[128:143]\n"
    "s_waitcnt vmcnt(8) lgkmcnt(0)\n"
    "s_barrier\n"
    "v_fma_f32 v47, v47, v16, v16\n"
    "v_rcp_f32 v44, v44\n"
    "v_mfma_f32_32x32x16_bf16 v[144:159], v[212:215], a[56:59], v[144:159]\n"
    "ds_read_b64_tr_b16 v[192:193], v3\n"
    "ds_read_b64_tr_b16 v[194:195], v3 offset:128\n"
    "ds_read_b64_tr_b16 v[196:197], v3 offset:32\n"
    "s_xor_b32 s86, s86, 0x80\n"
    "s_add_u32 m0, s89, 0x8900\n"
    "v_readlane_b32 s35, v245, 5\n"
    "v_mfma_f32_32x32x16_bf16 v[160:175], v[216:219], a[56:59], v[160:175]\n"
    "ds_read_b64_tr_b16 v[198:199], v3 offset:160\n"
    "ds_read_b64_tr_b16 v[200:201], v3 offset:64\n"
    "ds_read_b64_tr_b16 v[202:203], v3 offset:192\n"
    "buffer_load_dwordx4 v8, s[40:43], s86 offen lds\n"
    "v_rcp_f32 v45, v45\n"
    "v_mfma_f32_32x32x16_bf16 v[176:191], v[220:223], a[56:59], v[176:191]\n"
    "ds_read_b64_tr_b16 v[204:205], v3 offset:96\n"
    "ds_read_b64_tr_b16 v[206:207], v3 offset:224\n"
    "buffer_load_dwordx4 v9, s[44:47], s86 offen offset:1152 lds\n"
    "v_rcp_f32 v46, v46\n"
    "v_mfma_f32_32x32x16_bf16 v[128:143], v[224:227], a[60:63], v[128:143]\n"
    "ds_read_b64_tr_b16 v[208:209], v3 offset:512\n"
    "ds_read_b64_tr_b16 v[210:211], v3 offset:640\n"
    "v_rcp_f32 v47, v47\n"
    "v_mul_f32 v88, v88, v44\n"
    "v_mul_f32 v89, v89, v45\n"
    "v_mfma_f32_32x32x16_bf16 v[144:159], v[228:231], a[60:63], v[144:159]\n"
    "ds_read_b64_tr_b16 v[212:213], v3 offset:544\n"
    "ds_read_b64_tr_b16 v[214:215], v3 offset:672\n"
    "buffer_load_dwordx4 v10, s[40:43], s86 offen offset:2176 lds\n"
    "v_mul_f32 v90, v90, v46\n"
    "v_mul_f32 v91, v91, v47\n"
    "v_cvt_pk_bf16_f32 v80, v88, v89\n"
    "v_mfma_f32_32x32x16_bf16 v[160:175], v[232:235], a[60:63], v[160:175]\n"
    "ds_read_b64_tr_b16 v[216:217], v3 offset:576\n"
    "ds_read_b64_tr_b16 v[218:219], v3 offset:704\n"
    "v_cvt_pk_bf16_f32 v81, v90, v91\n"
    "v_mul_f32 v44, v84, v84\n"
    "v_mul_f32 v45, v85, v85\n"
    "v_mul_f32 v46, v86, v86\n"
    "v_mfma_f32_32x32x16_bf16 v[176:191], v[236:239], a[60:63], v[176:191]\n"
    "ds_read_b64_tr_b16 v[220:221], v3 offset:608\n"
    "ds_read_b64_tr_b16 v[222:223], v3 offset:736\n"
    "v_mul_f32 v47, v87, v87\n"
    "v_fma_f32 v44, v14, v44, v12\n"
    "v_fma_f32 v45, v14, v45, v12\n"
    "s_waitcnt lgkmcnt(8)\n"
    "v_mfma_f32_32x32x16_bf16 v[128:143], v[192:195], a[64:67], v[128:143]\n"
    "ds_read_b64_tr_b16 v[224:225], v3 offset:2176\n"
    "ds_read_b64_tr_b16 v[226:227], v3 offset:2304\n"
    "buffer_load_dwordx4 v11, s[44:47], s86 offen offset:3328 lds\n"
    "v_fma_f32 v46, v14, v46, v12\n"
    "v_fma_f32 v47, v14, v47, v12\n"
    "v_mul_f32 v44, v44, v84\n"
    "v_mfma_f32_32x32x16_bf16 v[144:159], v[196:199], a[64:67], v[144:159]\n"
    "ds_read_b64_tr_b16 v[228:229], v3 offset:2208\n"
    "ds_read_b64_tr_b16 v[230:231], v3 offset:2336\n"
    "v_mul_f32 v45, v45, v85\n"
    "v_mul_f32 v46, v46, v86\n"
    "v_mul_f32 v47, v47, v87\n"
    "v_mfma_f32_32x32x16_bf16 v[160:175], v[200:203], a[64:67], v[160:175]\n"
    "ds_read_b64_tr_b16 v[232:233], v3 offset:2240\n"
    "ds_read_b64_tr_b16 v[234:235], v3 offset:2368\n"
    "s_add_u32 m0, s90, 0\n"
    "s_nop 0\n"
    "buffer_load_dwordx4 v242, s[48:51], s35 offen lds\n"
    "v_exp_f32 v44, v44\n"
    "v_mfma_f32_32x32x16_bf16 v[176:191], v[204:207], a[64:67], v[176:191]\n"
    "ds_read_b64_tr_b16 v[236:237], v3 offset:2272\n"
    "ds_read_b64_tr_b16 v[238:239], v3 offset:2400\n"
    "v_exp_f32 v45, v45\n"
    "s_waitcnt lgkmcnt(8)\n"
    "v_mfma_f32_32x32x16_bf16 v[128:143], v[208:211], a[68:71], v[128:143]\n"
    "ds_read_b64_tr_b16 v[192:193], v3 offset:2688\n"
    "ds_read_b64_tr_b16 v[194:195], v3 offset:2816\n"
    "ds_read_b64_tr_b16 v[196:197], v3 offset:2720\n"
    "ds_read_b64_tr_b16 v[198:199], v3 offset:2848\n"
    "buffer_load_dwordx4 v243, s[48:51], s35 offen offset:1088 lds\n"
    "v_mfma_f32_32x32x16_bf16 v[144:159], v[212:215], a[68:71], v[144:159]\n"
    "ds_read_b64_tr_b16 v[200:201], v3 offset:2752\n"
    "ds_read_b64_tr_b16 v[202:203], v3 offset:2880\n"
    "ds_read_b64_tr_b16 v[204:205], v3 offset:2784\n"
    "ds_read_b64_tr_b16 v[206:207], v3 offset:2912\n"
    "v_exp_f32 v46, v46\n"
    "v_mfma_f32_32x32x16_bf16 v[160:175], v[216:219], a[68:71], v[160:175]\n"
    "v_exp_f32 v47, v47\n"
    "v_mul_f32 v92, v84, v92\n"
    "v_mul_f32 v93, v85, v93\n"
    "v_mul_f32 v94, v86, v94\n"
    "v_mul_f32 v95, v87, v95\n"
    "v_mfma_f32_32x32x16_bf16 v[176:191], v[220:223], a[68:71], v[176:191]\n"
    "v_fma_f32 v44, v44, v16, v16\n"
    "v_fma_f32 v45, v45, v16, v16\n"
    "v_fma_f32 v46, v46, v16, v16\n"
    "v_fma_f32 v47, v47, v16, v16\n"
    "s_waitcnt lgkmcnt(8)\n"
    "v_mfma_f32_32x32x16_bf16 v[128:143], v[224:227], a[72:75], v[128:143]\n"
    "s_waitcnt vmcnt(8) lgkmcnt(0)\n"
    "s_barrier\n"
    "v_rcp_f32 v44, v44\n"
    "v_rcp_f32 v45, v45\n"
    "v_mfma_f32_32x32x16_bf16 v[144:159], v[228:231], a[72:75], v[144:159]\n"
    "ds_read_b64_tr_b16 v[208:209], v3 offset:17536\n"
    "ds_read_b64_tr_b16 v[210:211], v3 offset:17664\n"
    "ds_read_b64_tr_b16 v[212:213], v3 offset:17568\n"
    "s_xor_b32 s87, s87, 0x80\n"
    "s_add_u32 m0, s89, 0\n"
    "v_readlane_b32 s98, v245, 6\n"
    "v_mfma_f32_32x32x16_bf16 v[160:175], v[232:235], a[72:75], v[160:175]\n"
    "ds_read_b64_tr_b16 v[214:215], v3 offset:17696\n"
    "ds_read_b64_tr_b16 v[216:217], v3 offset:17600\n"
    "ds_read_b64_tr_b16 v[218:219], v3 offset:17728\n"
    "buffer_load_dwordx4 v8, s[40:43], s87 offen lds\n"
    "v_rcp_f32 v46, v46\n"
    "v_mfma_f32_32x32x16_bf16 v[176:191], v[236:239], a[72:75], v[176:191]\n"
    "ds_read_b64_tr_b16 v[220:221], v3 offset:17632\n"
    "ds_read_b64_tr_b16 v[222:223], v3 offset:17760\n"
    "buffer_load_dwordx4 v9, s[44:47], s87 offen offset:1152 lds\n"
    "v_rcp_f32 v47, v47\n"
    "v_mfma_f32_32x32x16_bf16 v[128:143], v[192:195], a[76:79], v[128:143]\n"
    "ds_read_b64_tr_b16 v[224:225], v3 offset:18048\n"
    "ds_read_b64_tr_b16 v[226:227], v3 offset:18176\n"
    "v_mul_f32 v92, v92, v44\n"
    "v_mul_f32 v93, v93, v45\n"
    "v_mul_f32 v94, v94, v46\n"
    "v_mul_f32 v95, v95, v47\n"
    "v_mfma_f32_32x32x16_bf16 v[144:159], v[196:199], a[76:79], v[144:159]\n"
    "ds_read_b64_tr_b16 v[228:229], v3 offset:18080\n"
    "ds_read_b64_tr_b16 v[230:231], v3 offset:18208\n"
    "buffer_load_dwordx4 v10, s[40:43], s87 offen offset:2176 lds\n"
    "v_cvt_pk_bf16_f32 v82, v92, v93\n"
    "v_cvt_pk_bf16_f32 v83, v94, v95\n"
    "v_mul_f32 v44, v96, v96\n"
    "v_mfma_f32_32x32x16_bf16 v[160:175], v[200:203], a[76:79], v[160:175]\n"
    "ds_read_b64_tr_b16 v[232:233], v3 offset:18112\n"
    "ds_read_b64_tr_b16 v[234:235], v3 offset:18240\n"
    "v_mul_f32 v45, v97, v97\n"
    "v_mul_f32 v46, v98, v98\n"
    "v_mul_f32 v47, v99, v99\n"
    "v_fma_f32 v44, v14, v44, v12\n"
    "v_mfma_f32_32x32x16_bf16 v[176:191], v[204:207], a[76:79], v[176:191]\n"
    "ds_read_b64_tr_b16 v[236:237], v3 offset:18144\n"
    "ds_read_b64_tr_b16 v[238:239], v3 offset:18272\n"
    "v_fma_f32 v45, v14, v45, v12\n"
    "v_fma_f32 v46, v14, v46, v12\n"
    "v_fma_f32 v47, v14, v47, v12\n"
    "s_waitcnt lgkmcnt(8)\n"
    "v_mfma_f32_32x32x16_bf16 v[128:143], v[208:211], a[80:83], v[128:143]\n"
    "ds_read_b64_tr_b16 v[192:193], v3 offset:19712\n"
    "ds_read_b64_tr_b16 v[194:195], v3 offset:19840\n"
    "buffer_load_dwordx4 v11, s[44:47], s87 offen offset:3328 lds\n"
    "v_mul_f32 v44, v44, v96\n"
    "v_mul_f32 v45, v45, v97\n"
    "v_mul_f32 v46, v46, v98\n"
    "v_mfma_f32_32x32x16_bf16 v[144:159], v[212:215], a[80:83], v[144:159]\n"
    "ds_read_b64_tr_b16 v[196:197], v3 offset:19744\n"
    "ds_read_b64_tr_b16 v[198:199], v3 offset:19872\n"
    "v_mul_f32 v47, v47, v99\n"
    "v_exp_f32 v44, v44\n"
    "v_mfma_f32_32x32x16_bf16 v[160:175], v[216:219], a[80:83], v[160:175]\n"
    "ds_read_b64_tr_b16 v[200:201], v3 offset:19776\n"
    "ds_read_b64_tr_b16 v[202:203], v3 offset:19904\n"
    "s_add_u32 m0, s90, 0x880\n"
    "s_nop 0\n"
    "buffer_load_dwordx4 v242, s[48:51], s98 offen lds\n"
    "v_exp_f32 v45, v45\n"
    "v_mfma_f32_32x32x16_bf16 v[176:191], v[220:223], a[80:83], v[176:191]\n"
    "ds_read_b64_tr_b16 v[204:205], v3 offset:19808\n"
    "ds_read_b64_tr_b16 v[206:207], v3 offset:19936\n"
    "v_exp_f32 v46, v46\n"
    "s_waitcnt lgkmcnt(8)\n"
    "v_mfma_f32_32x32x16_bf16 v[128:143], v[224:227], a[84:87], v[128:143]\n"
    "ds_read_b64_tr_b16 v[208:209], v3 offset:20224\n"
    "ds_read_b64_tr_b16 v[210:211], v3 offset:20352\n"
    "ds_read_b64_tr_b16 v[212:213], v3 offset:20256\n"
    "ds_read_b64_tr_b16 v[214:215], v3 offset:20384\n"
    "buffer_load_dwordx4 v243, s[48:51], s98 offen offset:1088 lds\n"
    "v_mfma_f32_32x32x16_bf16 v[144:159], v[228:231], a[84:87], v[144:159]\n"
    "ds_read_b64_tr_b16 v[216:217], v3 offset:20288\n"
    "ds_read_b64_tr_b16 v[218:219], v3 offset:20416\n"
    "ds_read_b64_tr_b16 v[220:221], v3 offset:20320\n"
    "ds_read_b64_tr_b16 v[222:223], v3 offset:20448\n"
    "v_exp_f32 v47, v47\n"
    "v_mfma_f32_32x32x16_bf16 v[160:175], v[232:235], a[84:87], v[160:175]\n"
    "v_mul_f32 v104, v96, v104\n"
    "v_mul_f32 v105, v97, v105\n"
    "v_mul_f32 v106, v98, v106\n"
    "v_mul_f32 v107, v99, v107\n"
    "v_fma_f32 v44, v44, v16, v16\n"
    "v_fma_f32 v45, v45, v16, v16\n"
    "v_mfma_f32_32x32x16_bf16 v[176:191], v[236:239], a[84:87], v[176:191]\n"
    "v_fma_f32 v46, v46, v16, v16\n"
    "v_fma_f32 v47, v47, v16, v16\n"
    "v_rcp_f32 v44, v44\n"
    "s_waitcnt lgkmcnt(8)\n"
    "v_mfma_f32_32x32x16_bf16 v[128:143], v[192:195], a[88:91], v[128:143]\n"
    "s_waitcnt vmcnt(8) lgkmcnt(0)\n"
    "s_barrier\n"
    "v_rcp_f32 v45, v45\n"
    "v_rcp_f32 v46, v46\n"
    "v_mfma_f32_32x32x16_bf16 v[144:159], v[196:199], a[88:91], v[144:159]\n"
    "ds_read_b64_tr_b16 v[224:225], v3 offset:35072\n"
    "ds_read_b64_tr_b16 v[226:227], v3 offset:35200\n"
    "ds_read_b64_tr_b16 v[228:229], v3 offset:35104\n"
    "v_readlane_b32 s35, v245, 7\n"
    "buffer_load_dwordx2 v[254:255], v17, s[60:63], s24 offen sc1\n"
    "v_mfma_f32_32x32x16_bf16 v[160:175], v[200:203], a[88:91], v[160:175]\n"
    "ds_read_b64_tr_b16 v[230:231], v3 offset:35232\n"
    "ds_read_b64_tr_b16 v[232:233], v3 offset:35136\n"
    "ds_read_b64_tr_b16 v[234:235], v3 offset:35264\n"
    "v_rcp_f32 v47, v47\n"
    "v_mul_f32 v104, v104, v44\n"
    "v_mfma_f32_32x32x16_bf16 v[176:191], v[204:207], a[88:91], v[176:191]\n"
    "ds_read_b64_tr_b16 v[236:237], v3 offset:35168\n"
    "ds_read_b64_tr_b16 v[238:239], v3 offset:35296\n"
    "s_add_u32 m0, s69, 32\n"
    "s_nop 0\n"
    "buffer_load_dwordx4 v13, s[52:55], s31 offen lds\n"
    "v_mul_f32 v105, v105, v45\n"
    "v_mfma_f32_32x32x16_bf16 v[128:143], v[208:211], a[92:95], v[128:143]\n"
    "ds_read_b64_tr_b16 v[192:193], v3 offset:35584\n"
    "ds_read_b64_tr_b16 v[194:195], v3 offset:35712\n"
    "v_mul_f32 v106, v106, v46\n"
    "v_mul_f32 v107, v107, v47\n"
    "v_cvt_pk_bf16_f32 v96, v104, v105\n"
    "v_cvt_pk_bf16_f32 v97, v106, v107\n"
    "v_mfma_f32_32x32x16_bf16 v[144:159], v[212:215], a[92:95], v[144:159]\n"
    "ds_read_b64_tr_b16 v[196:197], v3 offset:35616\n"
    "ds_read_b64_tr_b16 v[198:199], v3 offset:35744\n"
    "v_mul_f32 v44, v100, v100\n"
    "v_mul_f32 v45, v101, v101\n"
    "v_mul_f32 v46, v102, v102\n"
    "v_mul_f32 v47, v103, v103\n"
    "v_mfma_f32_32x32x16_bf16 v[160:175], v[216:219], a[92:95], v[160:175]\n"
    "ds_read_b64_tr_b16 v[200:201], v3 offset:35648\n"
    "ds_read_b64_tr_b16 v[202:203], v3 offset:35776\n"
    "buffer_load_dwordx4 v13, s[52:55], s31 offen offset:1024 lds\n"
    "v_fma_f32 v44, v14, v44, v12\n"
    "v_fma_f32 v45, v14, v45, v12\n"
    "v_fma_f32 v46, v14, v46, v12\n"
    "v_mfma_f32_32x32x16_bf16 v[176:191], v[220:223], a[92:95], v[176:191]\n"
    "ds_read_b64_tr_b16 v[204:205], v3 offset:35680\n"
    "ds_read_b64_tr_b16 v[206:207], v3 offset:35808\n"
    "v_fma_f32 v47, v14, v47, v12\n"
    "v_mul_f32 v44, v44, v100\n"
    "v_mul_f32 v45, v45, v101\n"
    "s_waitcnt lgkmcnt(8)\n"
    "v_mfma_f32_32x32x16_bf16 v[128:143], v[224:227], a[96:99], v[128:143]\n"
    "ds_read_b64_tr_b16 v[208:209], v3 offset:37248\n"
    "ds_read_b64_tr_b16 v[210:211], v3 offset:37376\n"
    "v_mul_f32 v46, v46, v102\n"
    "v_mul_f32 v47, v47, v103\n"
    "v_exp_f32 v44, v44\n"
    "v_mfma_f32_32x32x16_bf16 v[144:159], v[228:231], a[96:99], v[144:159]\n"
    "ds_read_b64_tr_b16 v[212:213], v3 offset:37280\n"
    "ds_read_b64_tr_b16 v[214:215], v3 offset:37408\n"
    "s_add_u32 m0, s90, 0x1100\n"
    "v_readlane_b32 s98, v245, 8\n"
    "buffer_load_dwordx4 v242, s[48:51], s35 offen lds\n"
    "v_mfma_f32_32x32x16_bf16 v[160:175], v[232:235], a[96:99], v[160:175]\n"
    "ds_read_b64_tr_b16 v[216:217], v3 offset:37312\n"
    "ds_read_b64_tr_b16 v[218:219], v3 offset:37440\n"
    "v_exp_f32 v45, v45\n"
    "v_exp_f32 v46, v46\n"
    "v_mfma_f32_32x32x16_bf16 v[176:191], v[236:239], a[96:99], v[176:191]\n"
    "ds_read_b64_tr_b16 v[220:221], v3 offset:37344\n"
    "ds_read_b64_tr_b16 v[222:223], v3 offset:37472\n"
    "v_exp_f32 v47, v47\n"
    "v_mul_f32 v108, v100, v108\n"
    "s_waitcnt lgkmcnt(8)\n"
    "v_mfma_f32_32x32x16_bf16 v[128:143], v[192:195], a[100:103], v[128:143]\n"
    "ds_read_b64_tr_b16 v[224:225], v3 offset:37760\n"
    "ds_read_b64_tr_b16 v[226:227], v3 offset:37888\n"
    "ds_read_b64_tr_b16 v[228:229], v3 offset:37792\n"
    "ds_read_b64_tr_b16 v[230:231], v3 offset:37920\n"
    "buffer_load_dwordx4 v243, s[48:51], s35 offen offset:1088 lds\n"
    "v_mul_f32 v109, v101, v109\n"
    "v_mfma_f32_32x32x16_bf16 v[144:159], v[196:199], a[100:103], v[144:159]\n"
    "ds_read_b64_tr_b16 v[232:233], v3 offset:37824\n"
    "ds_read_b64_tr_b16 v[234:235], v3 offset:37952\n"
    "ds_read_b64_tr_b16 v[236:237], v3 offset:37856\n"
    "ds_read_b64_tr_b16 v[238:239], v3 offset:37984\n"
    "v_mul_f32 v110, v102, v110\n"
    "v_mul_f32 v111, v103, v111\n"
    "v_mfma_f32_32x32x16_bf16 v[160:175], v[200:203], a[100:103], v[160:175]\n"
    "v_fma_f32 v44, v44, v16, v16\n"
    "v_fma_f32 v45, v45, v16, v16\n"
    "v_fma_f32 v46, v46, v16, v16\n"
    "v_fma_f32 v47, v47, v16, v16\n"
    "v_rcp_f32 v44, v44\n"
    "v_mfma_f32_32x32x16_bf16 v[176:191], v[204:207], a[100:103], v[176:191]\n"
    "v_rcp_f32 v45, v45\n"
    "v_rcp_f32 v46, v46\n"
    "s_waitcnt lgkmcnt(8)\n"
    "v_mfma_f32_32x32x16_bf16 v[128:143], v[208:211], a[104:107], v[128:143]\n"
    "s_waitcnt vmcnt(4) lgkmcnt(0)\n"
    "s_barrier\n"
    "v_cmp_ne_u64 vcc, s[8:9], v[254:255]\n"
    "s_nop 1\n"
    "s_cbranch_vccnz .Lspin1_%=\n"
    ".Lret1_%=:\n"
    "v_mfma_f32_32x32x16_bf16 v[144:159], v[212:215], a[104:107], v[144:159]\n"
    "ds_read_b64_tr_b16 v[192:193], v3\n"
    "ds_read_b64_tr_b16 v[194:195], v3 offset:128\n"
    "ds_read_b64_tr_b16 v[196:197], v3 offset:32\n"
    "buffer_load_dwordx4 v[24:27], v13, s[60:63], s29 offen sc1\n"
    "v_rcp_f32 v47, v47\n"
    "v_mfma_f32_32x32x16_bf16 v[160:175], v[216:219], a[104:107], v[160:175]\n"
    "ds_read_b64_tr_b16 v[198:199], v3 offset:160\n"
    "ds_read_b64_tr_b16 v[200:201], v3 offset:64\n"
    "ds_read_b64_tr_b16 v[202:203], v3 offset:192\n"
    "v_mul_f32 v108, v108, v44\n"
    "v_mul_f32 v109, v109, v45\n"
    "v_mul_f32 v110, v110, v46\n"
    "v_mfma_f32_32x32x16_bf16 v[176:191], v[220:223], a[104:107], v[176:191]\n"
    "ds_read_b64_tr_b16 v[204:205], v3 offset:96\n"
    "ds_read_b64_tr_b16 v[206:207], v3 offset:224\n"
    "v_mul_f32 v111, v111, v47\n"
    "v_cvt_pk_bf16_f32 v98, v108, v109\n"
    "v_cvt_pk_bf16_f32 v99, v110, v111\n"
    "v_mfma_f32_32x32x16_bf16 v[128:143], v[224:227], a[108:111], v[128:143]\n"
    "ds_read_b64_tr_b16 v[208:209], v3 offset:512\n"
    "ds_read_b64_tr_b16 v[210:211], v3 offset:640\n"
    "buffer_load_dwordx4 v[28:31], v13, s[60:63], s29 offen offset:1024 sc1\n"
    "v_mul_f32 v44, v112, v112\n"
    "v_mul_f32 v45, v113, v113\n"
    "v_mul_f32 v46, v114, v114\n"
    "v_mfma_f32_32x32x16_bf16 v[144:159], v[228:231], a[108:111], v[144:159]\n"
    "ds_read_b64_tr_b16 v[212:213], v3 offset:544\n"
    "ds_read_b64_tr_b16 v[214:215], v3 offset:672\n"
    "v_mul_f32 v47, v115, v115\n"
    "v_fma_f32 v44, v14, v44, v12\n"
    "v_fma_f32 v45, v14, v45, v12\n"
    "v_fma_f32 v46, v14, v46, v12\n"
    "v_mfma_f32_32x32x16_bf16 v[160:175], v[232:235], a[108:111], v[160:175]\n"
    "ds_read_b64_tr_b16 v[216:217], v3 offset:576\n"
    "ds_read_b64_tr_b16 v[218:219], v3 offset:704\n"
    "v_fma_f32 v47, v14, v47, v12\n"
    "v_mul_f32 v44, v44, v112\n"
    "v_mul_f32 v45, v45, v113\n"
    "v_mul_f32 v46, v46, v114\n"
    "v_mfma_f32_32x32x16_bf16 v[176:191], v[236:239], a[108:111], v[176:191]\n"
    "ds_read_b64_tr_b16 v[220:221], v3 offset:608\n"
    "ds_read_b64_tr_b16 v[222:223], v3 offset:736\n"
    "v_mul_f32 v47, v47, v115\n"
    "v_exp_f32 v44, v44\n"
    "s_waitcnt lgkmcnt(8)\n"
    "v_mfma_f32_32x32x16_bf16 v[128:143], v[192:195], a[112:115], v[128:143]\n"
    "ds_read_b64_tr_b16 v[224:225], v3 offset:2176\n"
    "ds_read_b64_tr_b16 v[226:227], v3 offset:2304\n"
    "s_add_u32 m0, s90, 0x1980\n"
    "s_nop 0\n"
    "buffer_load_dwordx4 v242, s[48:51], s98 offen lds\n"
    "v_exp_f32 v45, v45\n"
    "v_mfma_f32_32x32x16_bf16 v[144:159], v[196:199], a[112:115], v[144:159]\n"
    "ds_read_b64_tr_b16 v[228:229], v3 offset:2208\n"
    "ds_read_b64_tr_b16 v[230:231], v3 offset:2336\n"
    "v_exp_f32 v46, v46\n"
    "v_exp_f32 v47, v47\n"
    "v_mfma_f32_32x32x16_bf16 v[160:175], v[200:203], a[112:115], v[160:175]\n"
    "ds_read_b64_tr_b16 v[232:233], v3 offset:2240\n"
    "ds_read_b64_tr_b16 v[234:235], v3 offset:2368\n"
    "v_mul_f32 v120, v112, v120\n"
    "v_mul_f32 v121, v113, v121\n"
    "v_mul_f32 v122, v114, v122\n"
    "v_mul_f32 v123, v115, v123\n"
    "v_mfma_f32_32x32x16_bf16 v[176:191], v[204:207], a[112:115], v[176:191]\n"
    "ds_read_b64_tr_b16 v[236:237], v3 offset:2272\n"
    "ds_read_b64_tr_b16 v[238:239], v3 offset:2400\n"
    "buffer_load_dwordx4 v243, s[48:51], s98 offen offset:1088 lds\n"
    "v_fma_f32 v44, v44, v16, v16\n"
    "v_fma_f32 v45, v45, v16, v16\n"
    "s_waitcnt lgkmcnt(8)\n"
    "v_mfma_f32_32x32x16_bf16 v[128:143], v[208:211], a[116:119], v[128:143]\n"
    "ds_read_b64_tr_b16 v[192:193], v3 offset:2688\n"
    "ds_read_b64_tr_b16 v[194:195], v3 offset:2816\n"
    "ds_read_b64_tr_b16 v[196:197], v3 offset:2720\n"
    "ds_read_b64_tr_b16 v[198:199], v3 offset:2848\n"
    "v_fma_f32 v46, v46, v16, v16\n"
    "v_fma_f32 v47, v47, v16, v16\n"
    "v_mfma_f32_32x32x16_bf16 v[144:159], v[212:215], a[116:119], v[144:159]\n"
    "ds_read_b64_tr_b16 v[200:201], v3 offset:2752\n"
    "ds_read_b64_tr_b16 v[202:203], v3 offset:2880\n"
    "ds_read_b64_tr_b16 v[204:205], v3 offset:2784\n"
    "ds_read_b64_tr_b16 v[206:207], v3 offset:2912\n"
    "v_rcp_f32 v44, v44\n"
    "v_mfma_f32_32x32x16_bf16 v[160:175], v[216:219], a[116:119], v[160:175]\n"
    "v_rcp_f32 v45, v45\n"
    "v_rcp_f32 v46, v46\n"
    "v_rcp_f32 v47, v47\n"
    "v_mfma_f32_32x32x16_bf16 v[176:191], v[220:223], a[116:119], v[176:191]\n"
    "v_mul_f32 v120, v120, v44\n"
    "v_mul_f32 v121, v121, v45\n"
    "v_mul_f32 v122, v122, v46\n"
    "v_mul_f32 v123, v123, v47\n"
    "v_cvt_pk_bf16_f32 v112, v120, v121\n"
    "s_waitcnt lgkmcnt(8)\n"
    "v_mfma_f32_32x32x16_bf16 v[128:143], v[224:227], a[120:123], v[128:143]\n"
    "v_cvt_pk_bf16_f32 v113, v122, v123\n"
    "v_mul_f32 v44, v116, v116\n"
    "v_mul_f32 v45, v117, v117\n"
    "v_mul_f32 v46, v118, v118\n"
    "v_mul_f32 v47, v119, v119\n"
    "v_fma_f32 v44, v14, v44, v12\n"
    "v_mfma_f32_32x32x16_bf16 v[144:159], v[228:231], a[120:123], v[144:159]\n"
    "v_fma_f32 v45, v14, v45, v12\n"
    "v_fma_f32 v46, v14, v46, v12\n"
    "v_fma_f32 v47, v14, v47, v12\n"
    "v_mul_f32 v44, v44, v116\n"
    "v_mul_f32 v45, v45, v117\n"
    "v_mul_f32 v46, v46, v118\n"
    "v_mfma_f32_32x32x16_bf16 v[160:175], v[232:235], a[120:123], v[160:175]\n"
    "v_mul_f32 v47, v47, v119\n"
    "v_exp_f32 v44, v44\n"
    "v_exp_f32 v45, v45\n"
    "v_mfma_f32_32x32x16_bf16 v[176:191], v[236:239], a[120:123], v[176:191]\n"
    "v_exp_f32 v46, v46\n"
    "v_exp_f32 v47, v47\n"
    "v_mul_f32 v124, v116, v124\n"
    "s_waitcnt lgkmcnt(0)\n"
    "v_mfma_f32_32x32x16_bf16 v[128:143], v[192:195], a[124:127], v[128:143]\n"
    "v_mul_f32 v125, v117, v125\n"
    "v_mul_f32 v126, v118, v126\n"
    "v_mul_f32 v127, v119, v127\n"
    "v_fma_f32 v44, v44, v16, v16\n"
    "v_fma_f32 v45, v45, v16, v16\n"
    "v_fma_f32 v46, v46, v16, v16\n"
    "v_mfma_f32_32x32x16_bf16 v[144:159], v[196:199], a[124:127], v[144:159]\n"
    "v_fma_f32 v47, v47, v16, v16\n"
    "v_rcp_f32 v44, v44\n"
    "v_rcp_f32 v45, v45\n"
    "v_mfma_f32_32x32x16_bf16 v[160:175], v[200:203], a[124:127], v[160:175]\n"
    "v_rcp_f32 v46, v46\n"
    "v_rcp_f32 v47, v47\n"
    "v_mul_f32 v124, v124, v44\n"
    "v_mul_f32 v125, v125, v45\n"
    "v_mfma_f32_32x32x16_bf16 v[176:191], v[204:207], a[124:127], v[176:191]\n"
    "v_mul_f32 v126, v126, v46\n"
    "v_mul_f32 v127, v127, v47\n"
    "v_cvt_pk_bf16_f32 v114, v124, v125\n"
    "v_cvt_pk_bf16_f32 v115, v126, v127\n"
    "v_mfma_f32_32x32x16_bf16 v[128:143], a[144:147], v[20:23], v[128:143]\n"
    "v_mfma_f32_32x32x16_bf16 v[144:159], a[148:151], v[20:23], v[144:159]\n"
    "v_mfma_f32_32x32x16_bf16 v[160:175], a[152:155], v[20:23], v[160:175]\n"
    "v_mfma_f32_32x32x16_bf16 v[176:191], a[156:159], v[20:23], v[176:191]\n"
    "buffer_store_dwordx4 v[64:67], v13, s[60:63], s93 offen sc1\n"
    "buffer_store_dwordx4 v[80:83], v13, s[60:63], s93 offen offset:1024 sc1\n"
    "buffer_store_dwordx4 v[96:99], v13, s[60:63], s93 offen offset:2048 sc1\n"
    "buffer_store_dwordx4 v[112:115], v13, s[60:63], s93 offen offset:3072 sc1\n"
    "s_xor_b32 s93, s93, 0x1000\n"
    "s_nop 3\n"
    "v_pk_mul_f32 v[192:193], v[128:129], v[128:129]\n"
    "v_pk_mul_f32 v[194:195], v[130:131], v[130:131]\n"
    "v_pk_mul_f32 v[196:197], v[132:133], v[132:133]\n"
    "v_pk_mul_f32 v[198:199], v[134:135], v[134:135]\n"
    "v_pk_fma_f32 v[192:193], v[14:15], v[192:193], v[12:13] op_sel_hi:[0,1,0]\n"
    "v_pk_fma_f32 v[194:195], v[14:15], v[194:195], v[12:13] op_sel_hi:[0,1,0]\n"
    "v_pk_fma_f32 v[196:197], v[14:15], v[196:197], v[12:13] op_sel_hi:[0,1,0]\n"
    "v_pk_fma_f32 v[198:199], v[14:15], v[198:199], v[12:13] op_sel_hi:[0,1,0]\n"
    "v_pk_mul_f32 v[192:193], v[192:193], v[128:129]\n"
    "v_pk_mul_f32 v[194:195], v[194:195], v[130:131]\n"
    "v_pk_mul_f32 v[196:197], v[196:197], v[132:133]\n"
    "v_pk_mul_f32 v[198:199], v[198:199], v[134:135]\n"
    "v_exp_f32 v192, v192\n"
    "v_exp_f32 v193, v193\n"
    "v_exp_f32 v194, v194\n"
    "v_exp_f32 v195, v195\n"
    "v_exp_f32 v196, v196\n"
    "v_exp_f32 v197, v197\n"
    "v_exp_f32 v198, v198\n"
    "v_exp_f32 v199, v199\n"
    "v_pk_mul_f32 v[136:137], v[128:129], v[136:137]\n"
    "v_pk_mul_f32 v[138:139], v[130:131], v[138:139]\n"
    "v_pk_mul_f32 v[140:141], v[132:133], v[140:141]\n"
    "v_pk_mul_f32 v[142:143], v[134:135], v[142:143]\n"
    "v_pk_fma_f32 v[192:193], v[192:193], v[16:17], v[16:17] op_sel_hi:[1,0,0]\n"
    "v_pk_fma_f32 v[194:195], v[194:195], v[16:17], v[16:17] op_sel_hi:[1,0,0]\n"
    "v_pk_fma_f32 v[196:197], v[196:197], v[16:17], v[16:17] op_sel_hi:[1,0,0]\n"
    "v_pk_fma_f32 v[198:199], v[198:199], v[16:17], v[16:17] op_sel_hi:[1,0,0]\n"
    "v_rcp_f32 v192, v192\n"
    "v_rcp_f32 v193, v193\n"
    "v_rcp_f32 v194, v194\n"
    "v_rcp_f32 v195, v195\n"
    "v_rcp_f32 v196, v196\n"
    "v_rcp_f32 v197, v197\n"
    "v_rcp_f32 v198, v198\n"
    "v_rcp_f32 v199, v199\n"
    "v_pk_mul_f32 v[136:137], v[136:137], v[192:193]\n"
    "v_pk_mul_f32 v[138:139], v[138:139], v[194:195]\n"
    "v_pk_mul_f32 v[140:141], v[140:141], v[196:197]\n"
    "v_pk_mul_f32 v[142:143], v[142:143], v[198:199]\n"
    "v_cvt_pk_bf16_f32 v128, v136, v137\n"
    "v_cvt_pk_bf16_f32 v129, v138, v139\n"
    "v_cvt_pk_bf16_f32 v130, v140, v141\n"
    "v_cvt_pk_bf16_f32 v131, v142, v143\n"
    "buffer_store_dwordx4 v[128:131], v13, s[60:63], s93 offen sc1\n"
    "v_pk_mul_f32 v[192:193], v[144:145], v[144:145]\n"
    "v_pk_mul_f32 v[194:195], v[146:147], v[146:147]\n"
    "v_pk_mul_f32 v[196:197], v[148:149], v[148:149]\n"
    "v_pk_mul_f32 v[198:199], v[150:151], v[150:151]\n"
    "v_pk_fma_f32 v[192:193], v[14:15], v[192:193], v[12:13] op_sel_hi:[0,1,0]\n"
    "v_pk_fma_f32 v[194:195], v[14:15], v[194:195], v[12:13] op_sel_hi:[0,1,0]\n"
    "v_pk_fma_f32 v[196:197], v[14:15], v[196:197], v[12:13] op_sel_hi:[0,1,0]\n"
    "v_pk_fma_f32 v[198:199], v[14:15], v[198:199], v[12:13] op_sel_hi:[0,1,0]\n"
    "v_pk_mul_f32 v[192:193], v[192:193], v[144:145]\n"
    "v_pk_mul_f32 v[194:195], v[194:195], v[146:147]\n"
    "v_pk_mul_f32 v[196:197], v[196:197], v[148:149]\n"
    "v_pk_mul_f32 v[198:199], v[198:199], v[150:151]\n"
    "v_exp_f32 v192, v192\n"
    "v_exp_f32 v193, v193\n"
    "v_exp_f32 v194, v194\n"
    "v_exp_f32 v195, v195\n"
    "v_exp_f32 v196, v196\n"
    "v_exp_f32 v197, v197\n"
    "v_exp_f32 v198, v198\n"
    "v_exp_f32 v199, v199\n"
    "v_pk_mul_f32 v[152:153], v[144:145], v[152:153]\n"
    "v_pk_mul_f32 v[154:155], v[146:147], v[154:155]\n"
    "v_pk_mul_f32 v[156:157], v[148:149], v[156:157]\n"
    "v_pk_mul_f32 v[158:159], v[150:151], v[158:159]\n"
    "v_pk_fma_f32 v[192:193], v[192:193], v[16:17], v[16:17] op_sel_hi:[1,0,0]\n"
    "v_pk_fma_f32 v[194:195], v[194:195], v[16:17], v[16:17] op_sel_hi:[1,0,0]\n"
    "v_pk_fma_f32 v[196:197], v[196:197], v[16:17], v[16:17] op_sel_hi:[1,0,0]\n"
    "v_pk_fma_f32 v[198:199], v[198:199], v[16:17], v[16:17] op_sel_hi:[1,0,0]\n"
    "v_rcp_f32 v192, v192\n"
    "v_rcp_f32 v193, v193\n"
    "v_rcp_f32 v194, v194\n"
    "v_rcp_f32 v195, v195\n"
    "v_rcp_f32 v196, v196\n"
    "v_rcp_f32 v197, v197\n"
    "v_rcp_f32 v198, v198\n"
    "v_rcp_f32 v199, v199\n"
    "v_pk_mul_f32 v[152:153], v[152:153], v[192:193]\n"
    "v_pk_mul_f32 v[154:155], v[154:155], v[194:195]\n"
    "v_pk_mul_f32 v[156:157], v[156:157], v[196:197]\n"
    "v_pk_mul_f32 v[158:159], v[158:159], v[198:199]\n"
    "v_cvt_pk_bf16_f32 v144, v152, v153\n"
    "v_cvt_pk_bf16_f32 v145, v154, v155\n"
    "v_cvt_pk_bf16_f32 v146, v156, v157\n"
    "v_cvt_pk_bf16_f32 v147, v158, v159\n"
    "buffer_store_dwordx4 v[144:147], v13, s[60:63], s93 offen offset:1024 sc1\n"
    "ds_write_b32 v59, v15\n"
    "s_waitcnt vmcnt(9)\n"
    "ds_write_b128 v58, v[24:27]\n"
    "s_waitcnt vmcnt(8)\n"
    "ds_write_b128 v58, v[28:31] offset:1152\n"
    "ds_write_b32 v60, v61\n"
    "v_pk_mul_f32 v[192:193], v[160:161], v[160:161]\n"
    "v_pk_mul_f32 v[194:195], v[162:163], v[162:163]\n"
    "v_pk_mul_f32 v[196:197], v[164:165], v[164:165]\n"
    "v_pk_mul_f32 v[198:199], v[166:167], v[166:167]\n"
    "v_pk_fma_f32 v[192:193], v[14:15], v[192:193], v[12:13] op_sel_hi:[0,1,0]\n"
    "v_pk_fma_f32 v[194:195], v[14:15], v[194:195], v[12:13] op_sel_hi:[0,1,0]\n"
    "v_pk_fma_f32 v[196:197], v[14:15], v[196:197], v[12:13] op_sel_hi:[0,1,0]\n"
    "v_pk_fma_f32 v[198:199], v[14:15], v[198:199], v[12:13] op_sel_hi:[0,1,0]\n"
    "v_pk_mul_f32 v[192:193], v[192:193], v[160:161]\n"
    "v_pk_mul_f32 v[194:195], v[194:195], v[162:163]\n"
    "v_pk_mul_f32 v[196:197], v[196:197], v[164:165]\n"
    "v_pk_mul_f32 v[198:199], v[198:199], v[166:167]\n"
    "v_exp_f32 v192, v192\n"
    "v_exp_f32 v193, v193\n"
    "v_exp_f32 v194, v194\n"
    "v_exp_f32 v195, v195\n"
    "v_exp_f32 v196, v196\n"
    "v_exp_f32 v197, v197\n"
    "v_exp_f32 v198, v198\n"
    "v_exp_f32 v199, v199\n"
    "v_pk_mul_f32 v[168:169], v[160:161], v[168:169]\n"
    "v_pk_mul_f32 v[170:171], v[162:163], v[170:171]\n"
    "v_pk_mul_f32 v[172:173], v[164:165], v[172:173]\n"
    "v_pk_mul_f32 v[174:175], v[166:167], v[174:175]\n"
    "v_pk_fma_f32 v[192:193], v[192:193], v[16:17], v[16:17] op_sel_hi:[1,0,0]\n"
    "v_pk_fma_f32 v[194:195], v[194:195], v[16:17], v[16:17] op_sel_hi:[1,0,0]\n"
    "v_pk_fma_f32 v[196:197], v[196:197], v[16:17], v[16:17] op_sel_hi:[1,0,0]\n"
    "v_pk_fma_f32 v[198:199], v[198:199], v[16:17], v[16:17] op_sel_hi:[1,0,0]\n"
    "v_rcp_f32 v192, v192\n"
    "v_rcp_f32 v193, v193\n"
    "v_rcp_f32 v194, v194\n"
    "v_rcp_f32 v195, v195\n"
    "v_rcp_f32 v196, v196\n"
    "v_rcp_f32 v197, v197\n"
    "v_rcp_f32 v198, v198\n"
    "v_rcp_f32 v199, v199\n"
    "v_pk_mul_f32 v[168:169], v[168:169], v[192:193]\n"
    "v_pk_mul_f32 v[170:171], v[170:171], v[194:195]\n"
    "v_pk_mul_f32 v[172:173], v[172:173], v[196:197]\n"
    "v_pk_mul_f32 v[174:175], v[174:175], v[198:199]\n"
    "v_cvt_pk_bf16_f32 v160, v168, v169\n"
    "v_cvt_pk_bf16_f32 v161, v170, v171\n"
    "v_cvt_pk_bf16_f32 v162, v172, v173\n"
    "v_cvt_pk_bf16_f32 v163, v174, v175\n"
    "buffer_store_dwordx4 v[160:163], v13, s[60:63], s93 offen offset:2048 sc1\n"
    "v_pk_mul_f32 v[192:193], v[176:177], v[176:177]\n"
    "v_pk_mul_f32 v[194:195], v[178:179], v[178:179]\n"
    "v_pk_mul_f32 v[196:197], v[180:181], v[180:181]\n"
    "v_pk_mul_f32 v[198:199], v[182:183], v[182:183]\n"
    "v_pk_fma_f32 v[192:193], v[14:15], v[192:193], v[12:13] op_sel_hi:[0,1,0]\n"
    "v_pk_fma_f32 v[194:195], v[14:15], v[194:195], v[12:13] op_sel_hi:[0,1,0]\n"
    "v_pk_fma_f32 v[196:197], v[14:15], v[196:197], v[12:13] op_sel_hi:[0,1,0]\n"
    "v_pk_fma_f32 v[198:199], v[14:15], v[198:199], v[12:13] op_sel_hi:[0,1,0]\n"
    "v_pk_mul_f32 v[192:193], v[192:193], v[176:177]\n"
    "v_pk_mul_f32 v[194:195], v[194:195], v[178:179]\n"
    "v_pk_mul_f32 v[196:197], v[196:197], v[180:181]\n"
    "v_pk_mul_f32 v[198:199], v[198:199], v[182:183]\n"
    "v_exp_f32 v192, v192\n"
    "v_exp_f32 v193, v193\n"
    "v_exp_f32 v194, v194\n"
    "v_exp_f32 v195, v195\n"
    "v_exp_f32 v196, v196\n"
    "v_exp_f32 v197, v197\n"
    "v_exp_f32 v198, v198\n"
    "v_exp_f32 v199, v199\n"
    "v_pk_mul_f32 v[184:185], v[176:177], v[184:185]\n"
    "v_pk_mul_f32 v[186:187], v[178:179], v[186:187]\n"
    "v_pk_mul_f32 v[188:189], v[180:181], v[188:189]\n"
    "v_pk_mul_f32 v[190:191], v[182:183], v[190:191]\n"
    "v_pk_fma_f32 v[192:193], v[192:193], v[16:17], v[16:17] op_sel_hi:[1,0,0]\n"
    "v_pk_fma_f32 v[194:195], v[194:195], v[16:17], v[16:17] op_sel_hi:[1,0,0]\n"
    "v_pk_fma_f32 v[196:197], v[196:197], v[16:17], v[16:17] op_sel_hi:[1,0,0]\n"
    "v_pk_fma_f32 v[198:199], v[198:199], v[16:17], v[16:17] op_sel_hi:[1,0,0]\n"
    "v_rcp_f32 v192, v192\n"
    "v_rcp_f32 v193, v193\n"
    "v_rcp_f32 v194, v194\n"
    "v_rcp_f32 v195, v195\n"
    "v_rcp_f32 v196, v196\n"
    "v_rcp_f32 v197, v197\n"
    "v_rcp_f32 v198, v198\n"
    "v_rcp_f32 v199, v199\n"
    "v_pk_mul_f32 v[184:185], v[184:185], v[192:193]\n"
    "v_pk_mul_f32 v[186:187], v[186:187], v[194:195]\n"
    "v_pk_mul_f32 v[188:189], v[188:189], v[196:197]\n"
    "v_pk_mul_f32 v[190:191], v[190:191], v[198:199]\n"
    "v_cvt_pk_bf16_f32 v176, v184, v185\n"
    "v_cvt_pk_bf16_f32 v177, v186, v187\n"
    "v_cvt_pk_bf16_f32 v178, v188, v189\n"
    "v_cvt_pk_bf16_f32 v179, v190, v191\n"
    "buffer_store_dwordx4 v[176:179], v13, s[60:63], s93 offen offset:3072 sc1\n"
    "s_waitcnt lgkmcnt(0)\n"
    "s_barrier\n"
    "s_branch .Lconv_%=\n"
    ".LB_%=:\n"
    "s_waitcnt lgkmcnt(0)\n"
    "s_sub_u32 s36, s36, 64\n"
    "s_subb_u32 s37, s37, 0\n"
    "s_and_b32 s37, s37, 0xffff\n"
    "s_mov_b32 s38, 0x200040\n"
    "s_mov_b32 s39, 0x20000\n"
    "s_and_b32 s61, s61, 0xffff\n"
    "s_mov_b32 s62, 0xc00000\n"
    "s_mov_b32 s63, 0x20000\n"
    "s_and_b32 s53, s53, 0xffff\n"
    "s_mov_b32 s54, 0x200000\n"
    "s_mov_b32 s55, 0x20000\n"
    "s_lshl_b32 s19, s13, 13\n"
    "s_lshr_b32 s33, s12, 1\n"
    "s_lshl_b32 s33, s33, 12\n"
    "s_add_u32 s19, s19, s33\n"
    "s_lshl_b32 s33, s13, 2\n"
    "v_mov_b32 v15, 0\n"
    "v_mov_b32 v49, s33\n"
    "buffer_load_dword v20, v15, s[52:55], s19 offen\n"
    "global_load_dword v21, v49, s[20:21]\n"
    "s_mov_b64 s[8:9], %[tag]\n"
    "v_and_b32 v128, 31, v1\n"
    "v_lshrrev_b32 v129, 5, v1\n"
    "v_bfe_u32 v130, v1, 2, 2\n"
    "v_and_b32 v131, 3, v1\n"
    "v_bfe_u32 v132, v1, 4, 1\n"
    "s_and_b32 s49, s49, 0xffff\n"
    "s_mov_b32 s50, 0x1c0000\n"
    "s_mov_b32 s51, 0x20000\n"
    "s_and_b32 s57, s57, 0xffff\n"
    "s_mov_b32 s58, 0x200000\n"
    "s_mov_b32 s59, 0x20000\n"
    "s_lshl_b32 s16, s14, 2\n"
    "s_lshr_b32 s17, s15, 3\n"
    "s_add_u32 s22, s16, s17\n"
    "s_and_b32 s23, s15, 7\n"
    "s_lshl_b32 s17, s17, 1\n"
    "s_mov_b32 s18, s23\n"
    "s_add_u32 s17, s17, s18\n"
    "s_and_b32 s17, s17, 7\n"
    "s_mul_i32 s17, s17, 7\n"
    "s_lshr_b32 s17, s17, 1\n"
    "s_add_u32 s17, s17, s14\n"
    "s_sub_u32 s18, s17, 28\n"
    "s_min_u32 s17, s17, s18\n"
    "v_add_u32 v133, s17, v1\n"
    "v_subrev_u32 v134, 28, v133\n"
    "v_cmp_gt_u32 vcc, 28, v133\n"
    "s_nop 1\n"
    "v_cndmask_b32 v133, v134, v133, vcc\n"
    "v_lshlrev_b32 v133, 2, v133\n"
    "v_add_u32 v133, s12, v133\n"
    "v_lshlrev_b32 v245, 14, v133\n"
    "s_mul_i32 s16, s22, 0x38000\n"
    "s_add_u32 s16, s16, 0x500000\n"
    "v_lshlrev_b32 v133, 10, v133\n"
    "v_add_u32 v244, s16, v133\n"
    "v_lshlrev_b32 v13, 4, v1\n"
    "v_lshlrev_b32 v17, 3, v131\n"
    "v_min_u32 v133, 13, v1\n"
    "v_lshlrev_b32 v18, 3, v133\n"
    "s_and_b32 s16, s12, 1\n"
    "s_mul_i32 s16, s16, 0x1c000\n"
    "v_add_u32 v246, s16, v13\n"
    "s_sub_u32 s16, 0x1c000, s16\n"
    "v_add_u32 v247, s16, v13\n"
    "v_lshrrev_b32 v133, 4, v1\n"
    "v_bfe_u32 v134, v1, 3, 1\n"
    "v_lshl_add_u32 v133, v133, 2, v134\n"
    "v_and_b32 v134, 7, v1\n"
    "v_lshlrev_b32 v134, 4, v134\n"
    "v_lshl_add_u32 v133, v133, 10, v134\n"
    "s_lshl_b32 s16, s23, 7\n"
    "v_add_u32 v242, s16, v133\n"
    "v_add_u32 v243, 0x3c0, v242\n"
    "s_mul_i32 s90, s12, 0x2a80\n"
    "s_add_u32 s91, s90, 0x1d4c0\n"
    "s_mul_i32 s92, s12, 0x2200\n"
    "s_add_u32 s92, s92, 0xc300\n"
    "v_lshrrev_b32 v133, 1, v130\n"
    "v_mul_u32_u24 v133, 0x440, v133\n"
    "v_lshl_add_u32 v133, v129, 8, v133\n"
    "v_and_b32 v134, 1, v130\n"
    "v_lshl_add_u32 v133, v134, 7, v133\n"
    "v_lshl_add_u32 v133, v132, 5, v133\n"
    "v_lshl_add_u32 v133, v131, 3, v133\n"
    "s_lshr_b32 s16, s12, 1\n"
    "s_lshl_b32 s16, s16, 6\n"
    "s_xor_b32 s18, s16, 64\n"
    "v_add_u32 v134, s18, v133\n"
    "v_add_u32 v133, s16, v133\n"
    "v_add_u32 v248, s90, v133\n"
    "v_add_u32 v249, s90, v134\n"
    "v_add_u32 v250, s91, v133\n"
    "v_add_u32 v251, s91, v134\n"
    "v_add_u32 v252, s92, v133\n"
    "v_add_u32 v253, s92, v134\n"
    "s_lshr_b32 s16, s13, 5\n"
    "s_lshl_b32 s18, s16, 7\n"
    "s_add_u32 s24, s18, 0x4000\n"
    "s_lshl_b32 s18, s13, 3\n"
    "s_add_u32 s28, s18, 0x5000\n"
    "s_lshr_b32 s18, s22, 1\n"
    "s_lshl_b32 s16, s18, 14\n"
    "s_add_u32 s68, s16, 0x10000\n"
    "s_lshl_b32 s18, s18, 7\n"
    "s_add_u32 s25, s18, 0x3000\n"
    "s_lshl_b32 s18, s23, 8\n"
    "s_add_u32 s26, s18, 0x5000\n"
    "s_lshl_b32 s18, s13, 13\n"
    "s_lshl_b32 s16, s12, 11\n"
    "s_add_u32 s31, s18, s16\n"
    "s_add_u32 s29, s31, 0x100000\n"
    "s_add_u32 s30, s31, 0x300000\n"
    "s_cmp_eq_u32 s12, 0\n"
    "s_cselect_b64 s[10:11], 3, 0\n"
    "s_and_b32 s19, s22, 1\n"
    "s_lshl_b32 s19, s19, 9\n"
    "s_and_b32 s16, s12, 1\n"
    "s_lshl_b32 s16, s16, 8\n"
    "s_add_u32 s19, s19, s16\n"
    "v_lshlrev_b32 v133, 3, v128\n"
    "v_lshl_add_u32 v133, v129, 12, v133\n"
    "v_add_u32 v35, s19, v133\n"
    "s_lshr_b32 s16, s12, 1\n"
    "s_mul_i32 s16, s16, 0x1020\n"
    "s_add_u32 s16, s16, 0x19080\n"
    "s_and_b32 s18, s12, 1\n"
    "s_lshl_b32 s18, s18, 11\n"
    "s_add_u32 s69, s16, s18\n"
    "s_mul_i32 s18, s12, 0x900\n"
    "s_add_u32 s18, s18, 0x1b0c0\n"
    "v_lshrrev_b32 v134, 4, v1\n"
    "v_and_b32 v135, 15, v1\n"
    "v_add_u32 v133, v130, v131\n"
    "v_lshlrev_b32 v133, 3, v133\n"
    "v_lshl_add_u32 v133, v134, 6, v133\n"
    "v_add_u32 v133, 8, v133\n"
    "v_add_u32 v56, s16, v133\n"
    "v_lshlrev_b32 v133, 3, v135\n"
    "v_sub_u32 v133, 0x78, v133\n"
    "v_lshl_add_u32 v133, v134, 4, v133\n"
    "v_add_u32 v57, s18, v133\n"
    "v_add_u32 v58, s18, v13\n"
    "v_mul_u32_u24 v133, 0x480, v129\n"
    "v_lshl_add_u32 v133, v128, 2, v133\n"
    "v_add_u32 v133, 0x400, v133\n"
    "v_add_u32 v59, s18, v133\n"
    "v_and_b32 v133, 7, v1\n"
    "v_lshlrev_b32 v133, 2, v133\n"
    "v_cmp_eq_u32 vcc, 0, v1\n"
    "s_nop 1\n"
    "v_cndmask_b32 v133, v133, 32, vcc\n"
    "v_add_u32 v60, s16, v133\n"
    "v_add_u32 v133, 47, v1\n"
    "v_cndmask_b32 v133, v133, 63, vcc\n"
    "v_subrev_u32 v50, 16, v1\n"
    "v_cmp_gt_u32 vcc, 16, v1\n"
    "s_nop 1\n"
    "v_cndmask_b32 v50, v50, v133, vcc\n"
    "v_lshlrev_b32 v50, 2, v50\n"
    "s_mul_i32 s16, s12, 0x2020\n"
    "s_add_u32 s16, s16, 0x14b00\n"
    "s_and_b32 s18, s12, 1\n"
    "v_lshlrev_b32 v134, 1, v134\n"
    "v_lshl_add_u32 v133, v135, 3, v134\n"
    "v_add_u32 v133, s18, v133\n"
    "v_bfe_u32 v134, v133, 4, 3\n"
    "v_xor_b32 v134, v133, v134\n"
    "v_lshlrev_b32 v134, 3, v134\n"
    "v_add_u32 v42, s16, v134\n"
    "v_add_u32 v133, 1, v133\n"
    "v_bfe_u32 v134, v133, 4, 3\n"
    "v_xor_b32 v134, v133, v134\n"
    "v_lshlrev_b32 v134, 3, v134\n"
    "v_add_u32 v43, s16, v134\n"
    "v_add_u32 v133, 1, v133\n"
    "s_cmp_eq_u32 s18, 0\n"
    "s_cselect_b32 s19, 0x2010, 0\n"
    "s_add_u32 s19, s19, s16\n"
    "v_and_b32 v133, 1, v1\n"
    "v_lshlrev_b32 v133, 2, v133\n"
    "v_add_u32 v51, s19, v133\n"
    "s_lshr_b32 s16, s12, 1\n"
    "s_mul_i32 s16, s16, 0x4040\n"
    "s_lshl_b32 s18, s18, 12\n"
    "s_add_u32 s16, s16, s18\n"
    "s_add_u32 s16, s16, 0x14b00\n"
    "v_bfe_u32 v134, v1, 2, 3\n"
    "v_lshlrev_b32 v133, 2, v1\n"
    "v_xor_b32 v133, v133, v134\n"
    "v_lshlrev_b32 v133, 3, v133\n"
    "v_add_u32 v38, s16, v133\n"
    "v_xor_b32 v134, 8, v133\n"
    "v_add_u32 v39, s16, v134\n"
    "v_xor_b32 v134, 16, v133\n"
    "v_add_u32 v40, s16, v134\n"
    "v_xor_b32 v134, 24, v133\n"
    "v_add_u32 v41, s16, v134\n"
    "s_mov_b32 s27, 0x6000\n"
    "s_add_u32 m0, s69, 32\n"
    "s_nop 0\n"
    "buffer_load_dwordx4 v13, s[52:55], s31 offen lds\n"
    "buffer_load_dwordx4 v13, s[52:55], s31 offen offset:1024 lds\n"
    "buffer_load_dwordx2 v[254:255], v17, s[60:63], s24 offen sc1\n"
    "v_readlane_b32 s98, v245, 0\n"
    "s_add_u32 m0, s91, 0\n"
    "s_nop 3\n"
    "buffer_load_dwordx4 v242, s[48:51], s98 offen lds\n"
    "buffer_load_dwordx4 v243, s[48:51], s98 offen offset:1088 lds\n"
    "v_readlane_b32 s35, v245, 1\n"
    "s_add_u32 m0, s91, 0x880\n"
    "s_nop 3\n"
    "buffer_load_dwordx4 v242, s[48:51], s35 offen lds\n"
    "buffer_load_dwordx4 v243, s[48:51], s35 offen offset:1088 lds\n"
    "v_readlane_b32 s98, v245, 2\n"
    "s_add_u32 m0, s91, 0x1100\n"
    "s_nop 3\n"
    "buffer_load_dwordx4 v242, s[48:51], s98 offen lds\n"
    "buffer_load_dwordx4 v243, s[48:51], s98 offen offset:1088 lds\n"
    "v_readlane_b32 s35, v245, 3\n"
    "s_add_u32 m0, s91, 0x1980\n"
    "s_nop 3\n"
    "buffer_load_dwordx4 v242, s[48:51], s35 offen lds\n"
    "buffer_load_dwordx4 v243, s[48:51], s35 offen offset:1088 lds\n"
    "v_readlane_b32 s98, v245, 4\n"
    "s_add_u32 m0, s91, 0x2200\n"
    "s_nop 3\n"
    "buffer_load_dwordx4 v242, s[48:51], s98 offen lds\n"
    "buffer_load_dwordx4 v243, s[48:51], s98 offen offset:1088 lds\n"
    "v_readlane_b32 s35, v245, 5\n"
    "s_add_u32 m0, s90, 0\n"
    "s_nop 3\n"
    "buffer_load_dwordx4 v242, s[48:51], s35 offen lds\n"
    "buffer_load_dwordx4 v243, s[48:51], s35 offen offset:1088 lds\n"
    "v_readlane_b32 s98, v245, 6\n"
    "s_add_u32 m0, s90, 0x880\n"
    "s_nop 3\n"
    "buffer_load_dwordx4 v242, s[48:51], s98 offen lds\n"
    "buffer_load_dwordx4 v243, s[48:51], s98 offen offset:1088 lds\n"
    "v_readlane_b32 s35, v245, 7\n"
    "s_add_u32 m0, s90, 0x1100\n"
    "s_nop 3\n"
    "buffer_load_dwordx4 v242, s[48:51], s35 offen lds\n"
    "buffer_load_dwordx4 v243, s[48:51], s35 offen offset:1088 lds\n"
    "v_readlane_b32 s98, v245, 8\n"
    "s_add_u32 m0, s90, 0x1980\n"
    "s_nop 3\n"
    "buffer_load_dwordx4 v242, s[48:51], s98 offen lds\n"
    "buffer_load_dwordx4 v243, s[48:51], s98 offen offset:1088 lds\n"
    "s_waitcnt vmcnt(22)\n"
    "v_readfirstlane_b32 s96, v20\n"
    "s_waitcnt vmcnt(21)\n"
    "v_readfirstlane_b32 s97, v21\n"
    "v_mov_b32 v62, s8\n"
    "v_mov_b32 v63, s9\n"
    "s_lshl_b32 s16, s97, 16\n"
    "s_and_b32 s18, s97, 0xffff0000\n"
    "s_cmp_lt_u32 s12, 2\n"
    "s_cselect_b32 s16, s16, s18\n"
    "s_lshl_b32 s18, s96, 16\n"
    "v_mov_b32 v133, s16\n"
    "v_add_f32 v133, s18, v133\n"
    "s_and_b32 s18, s96, 0xffff0000\n"
    "v_mov_b32 v134, s18\n"
    "v_cvt_pk_bf16_f32 v133, v133, v134\n"
    "v_cmp_eq_u32 vcc, 0, v1\n"
    "s_nop 1\n"
    "v_cndmask_b32 v61, 0, v133, vcc\n"
    "s_waitcnt vmcnt(18)\n"
    "v_cmp_ne_u64 vcc, s[8:9], v[254:255]\n"
    "s_nop 1\n"
    "s_cbranch_vccnz .Lspin2_%=\n"
    ".Lret2_%=:\n"
    "buffer_load_dwordx4 v[24:27], v13, s[60:63], s29 offen sc1\n"
    "buffer_load_dwordx4 v[28:31], v13, s[60:63], s29 offen offset:1024 sc1\n"
    "ds_write_b32 v59, v15\n"
    "s_waitcnt vmcnt(1)\n"
    "ds_write_b128 v58, v[24:27]\n"
    "s_waitcnt vmcnt(0)\n"
    "ds_write_b128 v58, v[28:31] offset:1152\n"
    "ds_write_b32 v60, v61\n"
    "s_waitcnt vmcnt(0) lgkmcnt(0)\n"
    "s_barrier\n"
    ".Lconv_%=:\n"
    "ds_read_b64_tr_b16 v[128:129], v56\n"
    "ds_read_b64_tr_b16 v[130:131], v56 offset:32\n"
    "ds_read_b64 v[192:193], v57\n"
    "ds_read_b64 v[194:195], v57 offset:8\n"
    "ds_read_b64 v[196:197], v57 offset:1152\n"
    "ds_read_b64 v[198:199], v57 offset:1160\n"
    "ds_read_b64_tr_b16 v[132:133], v56 offset:256\n"
    "ds_read_b64_tr_b16 v[134:135], v56 offset:288\n"
    "ds_read_b64 v[200:201], v57 offset:64\n"
    "ds_read_b64 v[202:203], v57 offset:72\n"
    "ds_read_b64 v[204:205], v57 offset:1216\n"
    "ds_read_b64 v[206:207], v57 offset:1224\n"
    "s_waitcnt lgkmcnt(8)\n"
    "v_mfma_f32_16x16x32_bf16 v[92:95], v[128:131], v[192:195], 0\n"
    "ds_read_b64_tr_b16 v[136:137], v56 offset:512\n"
    "ds_read_b64_tr_b16 v[138:139], v56 offset:544\n"
    "s_waitcnt lgkmcnt(8)\n"
    "v_mfma_f32_16x16x32_bf16 v[124:127], v[128:131], v[196:199], 0\n"
    "ds_read_b64 v[208:209], v57 offset:128\n"
    "ds_read_b64 v[210:211], v57 offset:136\n"
    "s_waitcnt lgkmcnt(6)\n"
    "v_mfma_f32_16x16x32_bf16 v[92:95], v[132:135], v[200:203], v[92:95]\n"
    "ds_read_b64 v[212:213], v57 offset:1280\n"
    "ds_read_b64 v[214:215], v57 offset:1288\n"
    "s_waitcnt lgkmcnt(6)\n"
    "v_mfma_f32_16x16x32_bf16 v[124:127], v[132:135], v[204:207], v[124:127]\n"
    "ds_read_b64_tr_b16 v[140:141], v56 offset:768\n"
    "ds_read_b64_tr_b16 v[142:143], v56 offset:800\n"
    "s_waitcnt lgkmcnt(4)\n"
    "v_mfma_f32_16x16x32_bf16 v[92:95], v[136:139], v[208:211], v[92:95]\n"
    "ds_read_b64 v[192:193], v57 offset:192\n"
    "ds_read_b64 v[194:195], v57 offset:200\n"
    "v_mfma_f32_16x16x32_bf16 v[88:91], v[128:131], v[208:211], 0\n"
    "ds_read_b64 v[196:197], v57 offset:1344\n"
    "ds_read_b64 v[198:199], v57 offset:1352\n"
    "s_waitcnt lgkmcnt(6)\n"
    "v_mfma_f32_16x16x32_bf16 v[124:127], v[136:139], v[212:215], v[124:127]\n"
    "ds_read_b64_tr_b16 v[144:145], v56 offset:1024\n"
    "ds_read_b64_tr_b16 v[146:147], v56 offset:1056\n"
    "v_mfma_f32_16x16x32_bf16 v[120:123], v[128:131], v[212:215], 0\n"
    "ds_read_b64 v[200:201], v57 offset:256\n"
    "ds_read_b64 v[202:203], v57 offset:264\n"
    "s_waitcnt lgkmcnt(6)\n"
    "v_mfma_f32_16x16x32_bf16 v[92:95], v[140:143], v[192:195], v[92:95]\n"
    "ds_read_b64 v[204:205], v57 offset:1408\n"
    "ds_read_b64 v[206:207], v57 offset:1416\n"
    "v_mfma_f32_16x16x32_bf16 v[88:91], v[132:135], v[192:195], v[88:91]\n"
    "ds_read_b64_tr_b16 v[148:149], v56 offset:1280\n"
    "ds_read_b64_tr_b16 v[150:151], v56 offset:1312\n"
    "s_waitcnt lgkmcnt(8)\n"
    "v_mfma_f32_16x16x32_bf16 v[124:127], v[140:143], v[196:199], v[124:127]\n"
    "ds_read_b64 v[208:209], v57 offset:320\n"
    "ds_read_b64 v[210:211], v57 offset:328\n"
    "v_mfma_f32_16x16x32_bf16 v[120:123], v[132:135], v[196:199], v[120:123]\n"
    "ds_read_b64 v[212:213], v57 offset:1472\n"
    "ds_read_b64 v[214:215], v57 offset:1480\n"
    "s_waitcnt lgkmcnt(8)\n"
    "v_mfma_f32_16x16x32_bf16 v[92:95], v[144:147], v[200:203], v[92:95]\n"
    "ds_read_b64_tr_b16 v[152:153], v56 offset:1536\n"
    "ds_read_b64_tr_b16 v[154:155], v56 offset:1568\n"
    "v_mfma_f32_16x16x32_bf16 v[88:91], v[136:139], v[200:203], v[88:91]\n"
    "ds_read_b64 v[192:193], v57 offset:384\n"
    "ds_read_b64 v[194:195], v57 offset:392\n"
    "v_mfma_f32_16x16x32_bf16 v[84:87], v[128:131], v[200:203], 0\n"
    "v_readlane_b32 s98, v245, 10\n"
    "v_readlane_b32 s35, v245, 9\n"
    "s_waitcnt lgkmcnt(10)\n"
    "v_mfma_f32_16x16x32_bf16 v[124:127], v[144:147], v[204:207], v[124:127]\n"
    "ds_read_b64 v[196:197], v57 offset:1536\n"
    "ds_read_b64 v[198:199], v57 offset:1544\n"
    "v_mfma_f32_16x16x32_bf16 v[120:123], v[136:139], v[204:207], v[120:123]\n"
    "s_add_u32 m0, s90, 0x2200\n"
    "s_nop 0\n"
    "buffer_load_dwordx4 v242, s[48:51], s35 offen lds\n"
    "v_mfma_f32_16x16x32_bf16 v[116:119], v[128:131], v[204:207], 0\n"
    "buffer_load_dwordx4 v243, s[48:51], s35 offen offset:1088 lds\n"
    "v_readlane_b32 s35, v245, 11\n"
    "s_waitcnt lgkmcnt(8)\n"
    "v_mfma_f32_16x16x32_bf16 v[92:95], v[148:151], v[208:211], v[92:95]\n"
    "ds_read_b64_tr_b16 v[156:157], v56 offset:1792\n"
    "ds_read_b64_tr_b16 v[158:159], v56 offset:1824\n"
    "v_mfma_f32_16x16x32_bf16 v[88:91], v[140:143], v[208:211], v[88:91]\n"
    "ds_read_b64 v[200:201], v57 offset:448\n"
    "ds_read_b64 v[202:203], v57 offset:456\n"
    "v_mfma_f32_16x16x32_bf16 v[84:87], v[132:135], v[208:211], v[84:87]\n"
    "s_add_u32 m0, s92, 0\n"
    "s_nop 0\n"
    "buffer_load_dwordx4 v242, s[48:51], s98 offen lds\n"
    "s_waitcnt lgkmcnt(10)\n"
    "v_mfma_f32_16x16x32_bf16 v[124:127], v[148:151], v[212:215], v[124:127]\n"
    "ds_read_b64 v[204:205], v57 offset:1600\n"
    "ds_read_b64 v[206:207], v57 offset:1608\n"
    "v_mfma_f32_16x16x32_bf16 v[120:123], v[140:143], v[212:215], v[120:123]\n"
    "buffer_load_dwordx4 v243, s[48:51], s98 offen offset:1088 lds\n"
    "v_readlane_b32 s98, v245, 12\n"
    "v_mfma_f32_16x16x32_bf16 v[116:119], v[132:135], v[212:215], v[116:119]\n"
    "s_add_u32 m0, s92, 0x880\n"
    "s_nop 0\n"
    "buffer_load_dwordx4 v242, s[48:51], s35 offen lds\n"
    "s_waitcnt lgkmcnt(8)\n"
    "v_mfma_f32_16x16x32_bf16 v[92:95], v[152:155], v[192:195], v[92:95]\n"
    "ds_read_b64_tr_b16 v[160:161], v56 offset:2048\n"
    "ds_read_b64_tr_b16 v[162:163], v56 offset:2080\n"
    "v_mfma_f32_16x16x32_bf16 v[88:91], v[144:147], v[192:195], v[88:91]\n"
    "ds_read_b64 v[208:209], v57 offset:512\n"
    "ds_read_b64 v[210:211], v57 offset:520\n"
    "v_mfma_f32_16x16x32_bf16 v[84:87], v[136:139], v[192:195], v[84:87]\n"
    "buffer_load_dwordx4 v243, s[48:51], s35 offen offset:1088 lds\n"
    "v_readlane_b32 s35, v245, 13\n"
    "v_mfma_f32_16x16x32_bf16 v[80:83], v[128:131], v[192:195], 0\n"
    "s_add_u32 m0, s92, 0x1100\n"
    "s_nop 0\n"
    "buffer_load_dwordx4 v242, s[48:51], s98 offen lds\n"
    "s_waitcnt lgkmcnt(10)\n"
    "v_mfma_f32_16x16x32_bf16 v[124:127], v[152:155], v[196:199], v[124:127]\n"
    "ds_read_b64 v[212:213], v57 offset:1664\n"
    "ds_read_b64 v[214:215], v57 offset:1672\n"
    "v_mfma_f32_16x16x32_bf16 v[120:123], v[144:147], v[196:199], v[120:123]\n"
    "buffer_load_dwordx4 v243, s[48:51], s98 offen offset:1088 lds\n"
    "s_add_u32 m0, s92, 0x1980\n"
    "v_mfma_f32_16x16x32_bf16 v[116:119], v[136:139], v[196:199], v[116:119]\n"
    "buffer_load_dwordx4 v242, s[48:51], s35 offen lds\n"
    "buffer_load_dwordx4 v243, s[48:51], s35 offen offset:1088 lds\n"
    "v_mfma_f32_16x16x32_bf16 v[112:115], v[128:131], v[196:199], 0\n"
    "ds_read_b64_tr_b16 a[112:113], v250\n"
    "s_waitcnt lgkmcnt(9)\n"
    "v_mfma_f32_16x16x32_bf16 v[92:95], v[156:159], v[200:203], v[92:95]\n"
    "ds_read_b64_tr_b16 v[164:165], v56 offset:2304\n"
    "ds_read_b64_tr_b16 v[166:167], v56 offset:2336\n"
    "v_mfma_f32_16x16x32_bf16 v[88:91], v[148:151], v[200:203], v[88:91]\n"
    "ds_read_b64 v[192:193], v57 offset:576\n"
    "ds_read_b64 v[194:195], v57 offset:584\n"
    "v_mfma_f32_16x16x32_bf16 v[84:87], v[140:143], v[200:203], v[84:87]\n"
    "ds_read_b64_tr_b16 a[114:115], v250 offset:512\n"
    "ds_read_b64_tr_b16 a[116:117], v251\n"
    "v_mfma_f32_16x16x32_bf16 v[80:83], v[132:135], v[200:203], v[80:83]\n"
    "ds_read_b64_tr_b16 a[118:119], v251 offset:512\n"
    "s_waitcnt lgkmcnt(14)\n"
    "v_mfma_f32_16x16x32_bf16 v[124:127], v[156:159], v[204:207], v[124:127]\n"
    "ds_read_b64 v[196:197], v57 offset:1728\n"
    "ds_read_b64 v[198:199], v57 offset:1736\n"
    "v_mfma_f32_16x16x32_bf16 v[120:123], v[148:151], v[204:207], v[120:123]\n"
    "ds_read_b64_tr_b16 a[120:121], v250 offset:2176\n"
    "v_readlane_b32 s98, v245, 14\n"
    "v_mfma_f32_16x16x32_bf16 v[116:119], v[140:143], v[204:207], v[116:119]\n"
    "ds_read_b64_tr_b16 a[122:123], v250 offset:2688\n"
    "ds_read_b64_tr_b16 a[124:125], v251 offset:2176\n"
    "v_mfma_f32_16x16x32_bf16 v[112:115], v[132:135], v[204:207], v[112:115]\n"
    "ds_read_b64_tr_b16 a[126:127], v251 offset:2688\n"
    "s_waitcnt lgkmcnt(6)\n"
    "v_mfma_f32_16x16x32_bf16 v[92:95], v[160:163], v[208:211], v[92:95]\n"
    "ds_read_b64_tr_b16 v[168:169], v56 offset:2560\n"
    "ds_read_b64_tr_b16 v[170:171], v56 offset:2592\n"
    "v_mfma_f32_16x16x32_bf16 v[88:91], v[152:155], v[208:211], v[88:91]\n"
    "ds_read_b64 v[200:201], v57 offset:640\n"
    "ds_read_b64 v[202:203], v57 offset:648\n"
    "v_mfma_f32_16x16x32_bf16 v[84:87], v[144:147], v[208:211], v[84:87]\n"
    "s_add_u32 m0, s91, 0\n"
    "s_nop 0\n"
    "buffer_load_dwordx4 v242, s[48:51], s98 offen lds\n"
    "v_mfma_f32_16x16x32_bf16 v[80:83], v[136:139], v[208:211], v[80:83]\n"
    "buffer_load_dwordx4 v243, s[48:51], s98 offen offset:1088 lds\n"
    "v_mfma_f32_16x16x32_bf16 v[76:79], v[128:131], v[208:211], 0\n"
    "ds_read_b64_tr_b16 a[128:129], v250 offset:4352\n"
    "v_readlane_b32 s35, v245, 15\n"
    "v_mfma_f32_16x16x32_bf16 v[124:127], v[160:163], v[212:215], v[124:127]\n"
    "ds_read_b64 v[204:205], v57 offset:1792\n"
    "ds_read_b64 v[206:207], v57 offset:1800\n"
    "v_mfma_f32_16x16x32_bf16 v[120:123], v[152:155], v[212:215], v[120:123]\n"
    "ds_read_b64_tr_b16 a[130:131], v250 offset:4864\n"
    "ds_read_b64_tr_b16 a[132:133], v251 offset:4352\n"
    "v_mfma_f32_16x16x32_bf16 v[116:119], v[144:147], v[212:215], v[116:119]\n"
    "ds_read_b64_tr_b16 a[134:135], v251 offset:4864\n"
    "s_waitcnt lgkmcnt(10)\n"
    "v_mfma_f32_16x16x32_bf16 v[112:115], v[136:139], v[212:215], v[112:115]\n"
    "s_add_u32 m0, s91, 0x880\n"
    "s_nop 0\n"
    "buffer_load_dwordx4 v242, s[48:51], s35 offen lds\n"
    "v_mfma_f32_16x16x32_bf16 v[108:111], v[128:131], v[212:215], 0\n"
    "buffer_load_dwordx4 v243, s[48:51], s35 offen offset:1088 lds\n"
    "v_mfma_f32_16x16x32_bf16 v[92:95], v[164:167], v[192:195], v[92:95]\n"
    "ds_read_b64_tr_b16 v[172:173], v56 offset:2816\n"
    "ds_read_b64_tr_b16 v[174:175], v56 offset:2848\n"
    "v_mfma_f32_16x16x32_bf16 v[88:91], v[156:159], v[192:195], v[88:91]\n"
    "ds_read_b64 v[208:209], v57 offset:704\n"
    "ds_read_b64 v[210:211], v57 offset:712\n"
    "v_mfma_f32_16x16x32_bf16 v[84:87], v[148:151], v[192:195], v[84:87]\n"
    "s_waitcnt vmcnt(14)\n"
    "s_barrier\n"
    "s_mov_b64 exec, s[10:11]\n"
    "buffer_store_dwordx2 v[62:63], v15, s[60:63], s27 offen sc1\n"
    "s_mov_b64 exec, -1\n"
    "s_nop 1\n"
    "v_mfma_f32_16x16x32_bf16 v[80:83], v[140:143], v[192:195], v[80:83]\n"
    "ds_read_b64_tr_b16 a[136:137], v250 offset:6528\n"
    "v_mfma_f32_16x16x32_bf16 v[76:79], v[132:135], v[192:195], v[76:79]\n"
    "v_readlane_b32 s98, v245, 16\n"
    "ds_read_b64_tr_b16 a[138:139], v250 offset:7040\n"
    "v_mfma_f32_16x16x32_bf16 v[124:127], v[164:167], v[196:199], v[124:127]\n"
    "ds_read_b64 v[212:213], v57 offset:1856\n"
    "ds_read_b64 v[214:215], v57 offset:1864\n"
    "v_mfma_f32_16x16x32_bf16 v[120:123], v[156:159], v[196:199], v[120:123]\n"
    "ds_read_b64_tr_b16 a[140:141], v251 offset:6528\n"
    "ds_read_b64_tr_b16 a[142:143], v251 offset:7040\n"
    "v_mfma_f32_16x16x32_bf16 v[116:119], v[148:151], v[196:199], v[116:119]\n"
    "s_waitcnt lgkmcnt(10)\n"
    "s_add_u32 m0, s91, 0x1100\n"
    "v_mfma_f32_16x16x32_bf16 v[112:115], v[140:143], v[196:199], v[112:115]\n"
    "buffer_load_dwordx4 v242, s[48:51], s98 offen lds\n"
    "buffer_load_dwordx4 v243, s[48:51], s98 offen offset:1088 lds\n"
    "v_mfma_f32_16x16x32_bf16 v[108:111], v[132:135], v[196:199], v[108:111]\n"
    "ds_read_b64_tr_b16 a[144:145], v250 offset:8704\n"
    "v_mfma_f32_16x16x32_bf16 v[92:95], v[168:171], v[200:203], v[92:95]\n"
    "ds_read_b64_tr_b16 v[176:177], v56 offset:3072\n"
    "ds_read_b64_tr_b16 v[178:179], v56 offset:3104\n"
    "v_mfma_f32_16x16x32_bf16 v[88:91], v[160:163], v[200:203], v[88:91]\n"
    "ds_read_b64 v[192:193], v57 offset:768\n"
    "ds_read_b64 v[194:195], v57 offset:776\n"
    "v_mfma_f32_16x16x32_bf16 v[84:87], v[152:155], v[200:203], v[84:87]\n"
    "v_readlane_b32 s35, v245, 17\n"
    "ds_read_b64_tr_b16 a[146:147], v250 offset:9216\n"
    "v_mfma_f32_16x16x32_bf16 v[80:83], v[144:147], v[200:203], v[80:83]\n"
    "ds_read_b64_tr_b16 a[148:149], v251 offset:8704\n"
    "ds_read_b64_tr_b16 a[150:151], v251 offset:9216\n"
    "v_mfma_f32_16x16x32_bf16 v[76:79], v[136:139], v[200:203], v[76:79]\n"
    "s_waitcnt lgkmcnt(8)\n"
    "s_add_u32 m0, s91, 0x1980\n"
    "v_mfma_f32_16x16x32_bf16 v[72:75], v[128:131], v[200:203], 0\n"
    "buffer_load_dwordx4 v242, s[48:51], s35 offen lds\n"
    "buffer_load_dwordx4 v243, s[48:51], s35 offen offset:1088 lds\n"
    "v_mfma_f32_16x16x32_bf16 v[124:127], v[168:171], v[204:207], v[124:127]\n"
    "ds_read_b64 v[196:197], v57 offset:1920\n"
    "ds_read_b64 v[198:199], v57 offset:1928\n"
    "v_mfma_f32_16x16x32_bf16 v[120:123], v[160:163], v[204:207], v[120:123]\n"
    "ds_read_b64_tr_b16 a[152:153], v248\n"
    "v_mfma_f32_16x16x32_bf16 v[116:119], v[152:155], v[204:207], v[116:119]\n"
    "v_readlane_b32 s98, v245, 18\n"
    "ds_read_b64_tr_b16 a[154:155], v248 offset:512\n"
    "v_mfma_f32_16x16x32_bf16 v[112:115], v[144:147], v[204:207], v[112:115]\n"
    "ds_read_b64_tr_b16 a[156:157], v249\n"
    "ds_read_b64_tr_b16 a[158:159], v249 offset:512\n"
    "v_mfma_f32_16x16x32_bf16 v[108:111], v[136:139], v[204:207], v[108:111]\n"
    "s_waitcnt lgkmcnt(6)\n"
    "s_add_u32 m0, s91, 0x2200\n"
    "v_mfma_f32_16x16x32_bf16 v[104:107], v[128:131], v[204:207], 0\n"
    "buffer_load_dwordx4 v242, s[48:51], s98 offen lds\n"
    "buffer_load_dwordx4 v243, s[48:51], s98 offen offset:1088 lds\n"
    "v_mfma_f32_16x16x32_bf16 v[92:95], v[172:175], v[208:211], v[92:95]\n"
    "ds_read_b64_tr_b16 v[180:181], v56 offset:3328\n"
    "ds_read_b64_tr_b16 v[182:183], v56 offset:3360\n"
    "v_mfma_f32_16x16x32_bf16 v[88:91], v[164:167], v[208:211], v[88:91]\n"
    "ds_read_b64 v[200:201], v57 offset:832\n"
    "ds_read_b64 v[202:203], v57 offset:840\n"
    "v_mfma_f32_16x16x32_bf16 v[84:87], v[156:159], v[208:211], v[84:87]\n"
    "ds_read_b64_tr_b16 a[160:161], v248 offset:2176\n"
    "v_mfma_f32_16x16x32_bf16 v[80:83], v[148:151], v[208:211], v[80:83]\n"
    "v_readlane_b32 s35, v245, 19\n"
    "ds_read_b64_tr_b16 a[162:163], v248 offset:2688\n"
    "v_mfma_f32_16x16x32_bf16 v[76:79], v[140:143], v[208:211], v[76:79]\n"
    "ds_read_b64_tr_b16 a[164:165], v249 offset:2176\n"
    "ds_read_b64_tr_b16 a[166:167], v249 offset:2688\n"
    "v_mfma_f32_16x16x32_bf16 v[72:75], v[132:135], v[208:211], v[72:75]\n"
    "s_waitcnt lgkmcnt(8)\n"
    "s_add_u32 m0, s90, 0\n"
    "v_mfma_f32_16x16x32_bf16 v[124:127], v[172:175], v[212:215], v[124:127]\n"
    "ds_read_b64 v[204:205], v57 offset:1984\n"
    "ds_read_b64 v[206:207], v57 offset:1992\n"
    "v_mfma_f32_16x16x32_bf16 v[120:123], v[164:167], v[212:215], v[120:123]\n"
    "buffer_load_dwordx4 v242, s[48:51], s35 offen lds\n"
    "buffer_load_dwordx4 v243, s[48:51], s35 offen offset:1088 lds\n"
    "v_mfma_f32_16x16x32_bf16 v[116:119], v[156:159], v[212:215], v[116:119]\n"
    "ds_read_b64_tr_b16 a[168:169], v248 offset:4352\n"
    "v_mfma_f32_16x16x32_bf16 v[112:115], v[148:151], v[212:215], v[112:115]\n"
    "v_readlane_b32 s98, v245, 20\n"
    "ds_read_b64_tr_b16 a[170:171], v248 offset:4864\n"
    "v_mfma_f32_16x16x32_bf16 v[108:111], v[140:143], v[212:215], v[108:111]\n"
    "ds_read_b64_tr_b16 a[172:173], v249 offset:4352\n"
    "ds_read_b64_tr_b16 a[174:175], v249 offset:4864\n"
    "v_mfma_f32_16x16x32_bf16 v[104:107], v[132:135], v[212:215], v[104:107]\n"
    "s_waitcnt lgkmcnt(6)\n"
    "s_add_u32 m0, s90, 0x880\n"
    "v_mfma_f32_16x16x32_bf16 v[92:95], v[176:179], v[192:195], v[92:95]\n"
    "ds_read_b64_tr_b16 v[184:185], v56 offset:3584\n"
    "ds_read_b64_tr_b16 v[186:187], v56 offset:3616\n"
    "v_mfma_f32_16x16x32_bf16 v[88:91], v[168:171], v[192:195], v[88:91]\n"
    "ds_read_b64 v[208:209], v57 offset:896\n"
    "ds_read_b64 v[210:211], v57 offset:904\n"
    "v_mfma_f32_16x16x32_bf16 v[84:87], v[160:163], v[192:195], v[84:87]\n"
    "buffer_load_dwordx4 v242, s[48:51], s98 offen lds\n"
    "buffer_load_dwordx4 v243, s[48:51], s98 offen offset:1088 lds\n"
    "v_mfma_f32_16x16x32_bf16 v[80:83], v[152:155], v[192:195], v[80:83]\n"
    "ds_read_b64_tr_b16 a[176:177], v248 offset:6528\n"
    "v_mfma_f32_16x16x32_bf16 v[76:79], v[144:147], v[192:195], v[76:79]\n"
    "v_readlane_b32 s35, v245, 21\n"
    "ds_read_b64_tr_b16 a[178:179], v248 offset:7040\n"
    "v_mfma_f32_16x16x32_bf16 v[72:75], v[136:139], v[192:195], v[72:75]\n"
    "ds_read_b64_tr_b16 a[180:181], v249 offset:6528\n"
    "ds_read_b64_tr_b16 a[182:183], v249 offset:7040\n"
    "v_mfma_f32_16x16x32_bf16 v[68:71], v[128:131], v[192:195], 0\n"
    "s_waitcnt lgkmcnt(8)\n"
    "s_add_u32 m0, s90, 0x1100\n"
    "v_mfma_f32_16x16x32_bf16 v[124:127], v[176:179], v[196:199], v[124:127]\n"
    "ds_read_b64 v[212:213], v57 offset:2048\n"
    "ds_read_b64 v[214:215], v57 offset:2056\n"
    "v_mfma_f32_16x16x32_bf16 v[120:123], v[168:171], v[196:199], v[120:123]\n"
    "buffer_load_dwordx4 v242, s[48:51], s35 offen lds\n"
    "buffer_load_dwordx4 v243, s[48:51], s35 offen offset:1088 lds\n"
    "v_mfma_f32_16x16x32_bf16 v[116:119], v[160:163], v[196:199], v[116:119]\n"
    "s_waitcnt vmcnt(24)\n"
    "ds_read_b64_tr_b16 a[184:185], v248 offset:8704\n"
    "v_mfma_f32_16x16x32_bf16 v[112:115], v[152:155], v[196:199], v[112:115]\n"
    "v_readlane_b32 s98, v245, 22\n"
    "ds_read_b64_tr_b16 a[186:187], v248 offset:9216\n"
    "v_mfma_f32_16x16x32_bf16 v[108:111], v[144:147], v[196:199], v[108:111]\n"
    "ds_read_b64_tr_b16 a[188:189], v249 offset:8704\n"
    "ds_read_b64_tr_b16 a[190:191], v249 offset:9216\n"
    "v_mfma_f32_16x16x32_bf16 v[104:107], v[136:139], v[196:199], v[104:107]\n"
    "s_waitcnt lgkmcnt(6)\n"
    "s_add_u32 m0, s90, 0x1980\n"
    "v_mfma_f32_16x16x32_bf16 v[100:103], v[128:131], v[196:199], 0\n"
    "buffer_load_dwordx4 v242, s[48:51], s98 offen lds\n"
    "buffer_load_dwordx4 v243, s[48:51], s98 offen offset:1088 lds\n"
    "v_mfma_f32_16x16x32_bf16 v[92:95], v[180:183], v[200:203], v[92:95]\n"
    "ds_read_b64_tr_b16 v[188:189], v56 offset:3840\n"
    "ds_read_b64_tr_b16 v[190:191], v56 offset:3872\n"
    "v_mfma_f32_16x16x32_bf16 v[88:91], v[172:175], v[200:203], v[88:91]\n"
    "ds_read_b64 v[192:193], v57 offset:960\n"
    "ds_read_b64 v[194:195], v57 offset:968\n"
    "v_mfma_f32_16x16x32_bf16 v[84:87], v[164:167], v[200:203], v[84:87]\n"
    "s_waitcnt vmcnt(24)\n"
    "ds_read_b64_tr_b16 a[192:193], v252\n"
    "v_mfma_f32_16x16x32_bf16 v[80:83], v[156:159], v[200:203], v[80:83]\n"
    "v_readlane_b32 s35, v245, 23\n"
    "ds_read_b64_tr_b16 a[194:195], v252 offset:512\n"
    "v_mfma_f32_16x16x32_bf16 v[76:79], v[148:151], v[200:203], v[76:79]\n"
    "ds_read_b64_tr_b16 a[196:197], v253\n"
    "ds_read_b64_tr_b16 a[198:199], v253 offset:512\n"
    "v_mfma_f32_16x16x32_bf16 v[72:75], v[140:143], v[200:203], v[72:75]\n"
    "s_waitcnt lgkmcnt(8)\n"
    "s_add_u32 m0, s90, 0x2200\n"
    "v_mfma_f32_16x16x32_bf16 v[68:71], v[132:135], v[200:203], v[68:71]\n"
    "buffer_load_dwordx4 v242, s[48:51], s35 offen lds\n"
    "buffer_load_dwordx4 v243, s[48:51], s35 offen offset:1088 lds\n"
    "v_mfma_f32_16x16x32_bf16 v[124:127], v[180:183], v[204:207], v[124:127]\n"
    "ds_read_b64 v[196:197], v57 offset:2112\n"
    "ds_read_b64 v[198:199], v57 offset:2120\n"
    "v_mfma_f32_16x16x32_bf16 v[120:123], v[172:175], v[204:207], v[120:123]\n"
    "s_waitcnt vmcnt(24)\n"
    "ds_read_b64_tr_b16 a[200:201], v252 offset:2176\n"
    "v_mfma_f32_16x16x32_bf16 v[116:119], v[164:167], v[204:207], v[116:119]\n"
    "v_readlane_b32 s98, v245, 24\n"
    "ds_read_b64_tr_b16 a[202:203], v252 offset:2688\n"
    "v_mfma_f32_16x16x32_bf16 v[112:115], v[156:159], v[204:207], v[112:115]\n"
    "ds_read_b64_tr_b16 a[204:205], v253 offset:2176\n"
    "ds_read_b64_tr_b16 a[206:207], v253 offset:2688\n"
    "v_mfma_f32_16x16x32_bf16 v[108:111], v[148:151], v[204:207], v[108:111]\n"
    "s_waitcnt lgkmcnt(6)\n"
    "s_add_u32 m0, s92, 0\n"
    "v_mfma_f32_16x16x32_bf16 v[104:107], v[140:143], v[204:207], v[104:107]\n"
    "buffer_load_dwordx4 v242, s[48:51], s98 offen lds\n"
    "buffer_load_dwordx4 v243, s[48:51], s98 offen offset:1088 lds\n"
    "v_mfma_f32_16x16x32_bf16 v[100:103], v[132:135], v[204:207], v[100:103]\n"
    "v_mfma_f32_16x16x32_bf16 v[92:95], v[184:187], v[208:211], v[92:95]\n"
    "s_waitcnt vmcnt(24)\n"
    "ds_read_b64_tr_b16 a[208:209], v252 offset:4352\n"
    "v_mfma_f32_16x16x32_bf16 v[88:91], v[176:179], v[208:211], v[88:91]\n"
    "ds_read_b64_tr_b16 a[210:211], v252 offset:4864\n"
    "v_readlane_b32 s35, v245, 25\n"
    "v_mfma_f32_16x16x32_bf16 v[84:87], v[168:171], v[208:211], v[84:87]\n"
    "ds_read_b64_tr_b16 a[212:213], v253 offset:4352\n"
    "ds_read_b64_tr_b16 a[214:215], v253 offset:4864\n"
    "v_mfma_f32_16x16x32_bf16 v[80:83], v[160:163], v[208:211], v[80:83]\n"
    "buffer_load_dwordx2 v[254:255], v18, s[60:63], s25 offen sc1\n"
    "v_mfma_f32_16x16x32_bf16 v[76:79], v[152:155], v[208:211], v[76:79]\n"
    "s_waitcnt lgkmcnt(4)\n"
    "s_add_u32 m0, s92, 0x880\n"
    "v_mfma_f32_16x16x32_bf16 v[72:75], v[144:147], v[208:211], v[72:75]\n"
    "buffer_load_dwordx4 v242, s[48:51], s35 offen lds\n"
    "buffer_load_dwordx4 v243, s[48:51], s35 offen offset:1088 lds\n"
    "v_mfma_f32_16x16x32_bf16 v[68:71], v[136:139], v[208:211], v[68:71]\n"
    "v_mfma_f32_16x16x32_bf16 v[64:67], v[128:131], v[208:211], 0\n"
    "v_mfma_f32_16x16x32_bf16 v[124:127], v[184:187], v[212:215], v[124:127]\n"
    "s_waitcnt vmcnt(25)\n"
    "ds_read_b64_tr_b16 a[216:217], v252 offset:6528\n"
    "v_mfma_f32_16x16x32_bf16 v[120:123], v[176:179], v[212:215], v[120:123]\n"
    "v_readlane_b32 s98, v245, 26\n"
    "ds_read_b64_tr_b16 a[218:219], v252 offset:7040\n"
    "v_mfma_f32_16x16x32_bf16 v[116:119], v[168:171], v[212:215], v[116:119]\n"
    "ds_read_b64_tr_b16 a[220:221], v253 offset:6528\n"
    "ds_read_b64_tr_b16 a[222:223], v253 offset:7040\n"
    "v_mfma_f32_16x16x32_bf16 v[112:115], v[160:163], v[212:215], v[112:115]\n"
    "v_mfma_f32_16x16x32_bf16 v[108:111], v[152:155], v[212:215], v[108:111]\n"
    "s_waitcnt lgkmcnt(4)\n"
    "v_readlane_b32 s35, v245, 27\n"
    "v_mfma_f32_16x16x32_bf16 v[104:107], v[144:147], v[212:215], v[104:107]\n"
    "s_add_u32 m0, s92, 0x1100\n"
    "s_nop 0\n"
    "buffer_load_dwordx4 v242, s[48:51], s98 offen lds\n"
    "v_mfma_f32_16x16x32_bf16 v[100:103], v[136:139], v[212:215], v[100:103]\n"
    "buffer_load_dwordx4 v243, s[48:51], s98 offen offset:1088 lds\n"
    "v_mfma_f32_16x16x32_bf16 v[96:99], v[128:131], v[212:215], 0\n"
    "v_mfma_f32_16x16x32_bf16 v[92:95], v[188:191], v[192:195], v[92:95]\n"
    "v_mfma_f32_16x16x32_bf16 v[88:91], v[180:183], v[192:195], v[88:91]\n"
    "v_mfma_f32_16x16x32_bf16 v[84:87], v[172:175], v[192:195], v[84:87]\n"
    "v_mfma_f32_16x16x32_bf16 v[80:83], v[164:167], v[192:195], v[80:83]\n"
    "v_mfma_f32_16x16x32_bf16 v[76:79], v[156:159], v[192:195], v[76:79]\n"
    "s_waitcnt lgkmcnt(0)\n"
    "s_add_u32 m0, s92, 0x1980\n"
    "v_mfma_f32_16x16x32_bf16 v[72:75], v[148:151], v[192:195], v[72:75]\n"
    "buffer_load_dwordx4 v242, s[48:51], s35 offen lds\n"
    "buffer_load_dwordx4 v243, s[48:51], s35 offen offset:1088 lds\n"
    "v_mfma_f32_16x16x32_bf16 v[68:71], v[140:143], v[192:195], v[68:71]\n"
    "v_mfma_f32_16x16x32_bf16 v[64:67], v[132:135], v[192:195], v[64:67]\n"
    "v_mfma_f32_16x16x32_bf16 v[124:127], v[188:191], v[196:199], v[124:127]\n"
    "v_mfma_f32_16x16x32_bf16 v[120:123], v[180:183], v[196:199], v[120:123]\n"
    "v_mfma_f32_16x16x32_bf16 v[116:119], v[172:175], v[196:199], v[116:119]\n"
    "v_mfma_f32_16x16x32_bf16 v[112:115], v[164:167], v[196:199], v[112:115]\n"
    "v_mfma_f32_16x16x32_bf16 v[108:111], v[156:159], v[196:199], v[108:111]\n"
    "v_mfma_f32_16x16x32_bf16 v[104:107], v[148:151], v[196:199], v[104:107]\n"
    "v_mfma_f32_16x16x32_bf16 v[100:103], v[140:143], v[196:199], v[100:103]\n"
    "v_mfma_f32_16x16x32_bf16 v[96:99], v[132:135], v[196:199], v[96:99]\n"
    "s_waitcnt vmcnt(6)\n"
    "v_cmp_ne_u64 vcc, s[8:9], v[254:255]\n"
    "s_nop 1\n"
    "s_cbranch_vccnz .Lspin3_%=\n"
    ".Lret3_%=:\n"
    "v_cmp_eq_u32 vcc, 63, v1\n"
    "s_nop 1\n"
    "v_cndmask_b32 v128, v99, v15, vcc\n"
    "v_cndmask_b32 v129, v103, v99, vcc\n"
    "v_cndmask_b32 v130, v107, v103, vcc\n"
    "v_cndmask_b32 v131, v111, v107, vcc\n"
    "buffer_load_dwordx2 v[24:25], v35, s[60:63], s68 offen sc1\n"
    "v_cndmask_b32 v132, v115, v111, vcc\n"
    "v_cndmask_b32 v133, v119, v115, vcc\n"
    "v_cndmask_b32 v134, v123, v119, vcc\n"
    "v_cndmask_b32 v135, v127, v123, vcc\n"
    "ds_bpermute_b32 v136, v50, v128\n"
    "v_readlane_b32 s99, v244, 0\n"
    "ds_bpermute_b32 v137, v50, v129\n"
    "ds_bpermute_b32 v138, v50, v130\n"
    "ds_bpermute_b32 v139, v50, v131\n"
    "ds_bpermute_b32 v140, v50, v132\n"
    "ds_bpermute_b32 v141, v50, v133\n"
    "buffer_load_dwordx2 v[26:27], v35, s[60:63], s68 offen offset:1024 sc1\n"
    "ds_bpermute_b32 v142, v50, v134\n"
    "ds_bpermute_b32 v143, v50, v135\n"
    "v_add_f32 v67, v67, v98\n"
    "v_add_f32 v66, v66, v97\n"
    "v_add_f32 v65, v65, v96\n"
    "buffer_load_dwordx2 v[28:29], v35, s[60:63], s68 offen offset:2048 sc1\n"
    "v_add_f32 v71, v71, v102\n"
    "v_add_f32 v70, v70, v101\n"
    "v_add_f32 v69, v69, v100\n"
    "v_add_f32 v75, v75, v106\n"
    "v_add_f32 v74, v74, v105\n"
    "v_readlane_b32 s67, v244, 1\n"
    "v_add_f32 v73, v73, v104\n"
    "v_add_f32 v79, v79, v110\n"
    "v_add_f32 v78, v78, v109\n"
    "v_add_f32 v77, v77, v108\n"
    "v_add_f32 v83, v83, v114\n"
    "buffer_load_dwordx2 v[30:31], v35, s[60:63], s68 offen offset:3072 sc1\n"
    "v_add_f32 v82, v82, v113\n"
    "v_add_f32 v81, v81, v112\n"
    "v_add_f32 v87, v87, v118\n"
    "v_add_f32 v86, v86, v117\n"
    "v_add_f32 v85, v85, v116\n"
    "buffer_load_dwordx4 a[0:3], v246, s[60:63], s99 offen sc1\n"
    "v_add_f32 v91, v91, v122\n"
    "v_add_f32 v90, v90, v121\n"
    "v_add_f32 v89, v89, v120\n"
    "v_add_f32 v95, v95, v126\n"
    "v_add_f32 v94, v94, v125\n"
    "buffer_load_dwordx4 a[4:7], v247, s[60:63], s99 offen sc1\n"
    "v_add_f32 v93, v93, v124\n"
    "s_barrier\n"
    "ds_write_b32 v51, v15\n"
    "s_waitcnt lgkmcnt(8)\n"
    "v_add_f32 v64, v64, v136\n"
    "ds_write_b64 v43, v[66:67]\n"
    "v_readlane_b32 s99, v244, 2\n"
    "ds_write_b64 v42, v[64:65]\n"
    "s_waitcnt lgkmcnt(9)\n"
    "v_add_f32 v68, v68, v137\n"
    "ds_write_b64 v43, v[70:71] offset:1024\n"
    "ds_write_b64 v42, v[68:69] offset:1024\n"
    "s_waitcnt lgkmcnt(10)\n"
    "v_add_f32 v72, v72, v138\n"
    "buffer_load_dwordx4 a[8:11], v246, s[60:63], s67 offen sc1\n"
    "ds_write_b64 v43, v[74:75] offset:2048\n"
    "ds_write_b64 v42, v[72:73] offset:2048\n"
    "s_waitcnt lgkmcnt(11)\n"
    "v_add_f32 v76, v76, v139\n"
    "ds_write_b64 v43, v[78:79] offset:3072\n"
    "ds_write_b64 v42, v[76:77] offset:3072\n"
    "buffer_load_dwordx4 a[12:15], v247, s[60:63], s67 offen sc1\n"
    "s_waitcnt lgkmcnt(12)\n"
    "v_add_f32 v80, v80, v140\n"
    "ds_write_b64 v43, v[82:83] offset:4096\n"
    "ds_write_b64 v42, v[80:81] offset:4096\n"
    "s_waitcnt lgkmcnt(13)\n"
    "v_add_f32 v84, v84, v141\n"
    "ds_write_b64 v43, v[86:87] offset:5120\n"
    "v_readlane_b32 s67, v244, 3\n"
    "ds_write_b64 v42, v[84:85] offset:5120\n"
    "s_waitcnt lgkmcnt(14)\n"
    "v_add_f32 v88, v88, v142\n"
    "ds_write_b64 v43, v[90:91] offset:6144\n"
    "ds_write_b64 v42, v[88:89] offset:6144\n"
    "s_waitcnt lgkmcnt(15)\n"
    "v_add_f32 v92, v92, v143\n"
    "buffer_load_dwordx4 a[16:19], v246, s[60:63], s99 offen sc1\n"
    "ds_write_b64 v43, v[94:95] offset:7168\n"
    "ds_write_b64 v42, v[92:93] offset:7168\n"
    "s_waitcnt lgkmcnt(0)\n"
    "s_barrier\n"
    "ds_read_b64 v[128:129], v38\n"
    "buffer_load_dwordx4 a[20:23], v247, s[60:63], s99 offen sc1\n"
    "ds_read_b64 v[144:145], v38 offset:8224\n"
    "ds_read_b64 v[130:131], v39\n"
    "ds_read_b64 v[146:147], v39 offset:8224\n"
    "ds_read_b64 v[132:133], v40\n"
    "ds_read_b64 v[148:149], v40 offset:8224\n"
    "v_readlane_b32 s99, v244, 4\n"
    "ds_read_b64 v[134:135], v41\n"
    "ds_read_b64 v[150:151], v41 offset:8224\n"
    "ds_read_b64 v[136:137], v38 offset:2048\n"
    "ds_read_b64 v[152:153], v38 offset:10272\n"
    "ds_read_b64 v[138:139], v39 offset:2048\n"
    "buffer_load_dwordx4 a[24:27], v246, s[60:63], s67 offen sc1\n"
    "ds_read_b64 v[154:155], v39 offset:10272\n"
    "ds_read_b64 v[140:141], v40 offset:2048\n"
    "ds_read_b64 v[156:157], v40 offset:10272\n"
    "ds_read_b64 v[142:143], v41 offset:2048\n"
    "ds_read_b64 v[158:159], v41 offset:10272\n"
    "buffer_load_dwordx4 a[28:31], v247, s[60:63], s67 offen sc1\n"
    "s_waitcnt lgkmcnt(14)\n"
    "v_pk_add_f32 v[128:129], v[128:129], v[144:145]\n"
    "s_waitcnt lgkmcnt(12)\n"
    "v_pk_add_f32 v[130:131], v[130:131], v[146:147]\n"
    "v_cvt_pk_bf16_f32 v160, v128, v129\n"
    "v_cvt_pk_bf16_f32 v161, v130, v131\n"
    "s_waitcnt lgkmcnt(10)\n"
    "v_pk_add_f32 v[132:133], v[132:133], v[148:149]\n"
    "v_readlane_b32 s67, v244, 5\n"
    "s_waitcnt lgkmcnt(8)\n"
    "v_pk_add_f32 v[134:135], v[134:135], v[150:151]\n"
    "v_cvt_pk_bf16_f32 v162, v132, v133\n"
    "v_cvt_pk_bf16_f32 v163, v134, v135\n"
    "s_waitcnt lgkmcnt(6)\n"
    "v_pk_add_f32 v[136:137], v[136:137], v[152:153]\n"
    "s_waitcnt lgkmcnt(4)\n"
    "v_pk_add_f32 v[138:139], v[138:139], v[154:155]\n"
    "buffer_load_dwordx4 a[32:35], v246, s[60:63], s99 offen sc1\n"
    "v_cvt_pk_bf16_f32 v164, v136, v137\n"
    "v_cvt_pk_bf16_f32 v165, v138, v139\n"
    "s_waitcnt lgkmcnt(2)\n"
    "v_pk_add_f32 v[140:141], v[140:141], v[156:157]\n"
    "s_waitcnt lgkmcnt(0)\n"
    "v_pk_add_f32 v[142:143], v[142:143], v[158:159]\n"
    "v_cvt_pk_bf16_f32 v166, v140, v141\n"
    "buffer_load_dwordx4 a[36:39], v247, s[60:63], s99 offen sc1\n"
    "v_cvt_pk_bf16_f32 v167, v142, v143\n"
    "buffer_store_dwordx4 v[160:163], v13, s[60:63], s30 offen sc1\n"
    "buffer_store_dwordx4 v[164:167], v13, s[60:63], s30 offen offset:1024 sc1\n"
    "s_waitcnt vmcnt(10)\n"
    "v_mfma_f32_32x32x16_bf16 v[64:79], a[112:115], a[0:3], 0\n"
    "v_readlane_b32 s99, v244, 6\n"
    "buffer_load_dwordx4 a[40:43], v246, s[60:63], s67 offen sc1\n"
    "v_and_b32 v128, 31, v1\n"
    "v_lshrrev_b32 v129, 5, v1\n"
    "v_bfe_u32 v130, v1, 2, 2\n"
    "v_and_b32 v131, 3, v1\n"
    "v_mfma_f32_32x32x16_bf16 v[96:111], a[112:115], a[4:7], 0\n"
    "buffer_load_dwordx4 a[44:47], v247, s[60:63], s67 offen sc1\n"
    "v_bfe_u32 v132, v1, 4, 1\n"
    "v_lshlrev_b32 v22, 3, v128\n"
    "v_lshrrev_b32 v133, 3, v1\n"
    "v_and_b32 v134, 7, v1\n"
    "v_lshlrev_b32 v134, 4, v134\n"
    "v_mfma_f32_32x32x16_bf16 v[80:95], a[116:119], a[0:3], 0\n"
    "v_readlane_b32 s67, v244, 7\n"
    "buffer_load_dwordx4 a[48:51], v246, s[60:63], s99 offen sc1\n"
    "v_lshl_add_u32 v2, v133, 12, v134\n"
    "s_lshl_b32 s16, s12, 3\n"
    "v_add_u32 v135, s16, v133\n"
    "v_mul_u32_u24 v135, 0x90, v135\n"
    "v_mfma_f32_32x32x16_bf16 v[112:127], a[116:119], a[4:7], 0\n"
    "buffer_load_dwordx4 a[52:55], v247, s[60:63], s99 offen sc1\n"
    "v_add_u32 v135, v135, v134\n"
    "v_add_u32 v3, 0x14b00, v135\n"
    "s_lshl_b32 s18, s23, 6\n"
    "s_add_u32 s16, s16, s18\n"
    "s_waitcnt vmcnt(12)\n"
    "v_mfma_f32_32x32x16_bf16 v[64:79], a[120:123], a[8:11], v[64:79]\n"
    "v_readlane_b32 s99, v244, 8\n"
    "buffer_load_dwordx4 a[56:59], v246, s[60:63], s67 offen sc1\n"
    "s_lshl_b32 s16, s16, 12\n"
    "s_lshl_b32 s18, s22, 7\n"
    "s_add_u32 s16, s16, s18\n"
    "s_add_u32 s72, s16, 0x300000\n"
    "v_mfma_f32_32x32x16_bf16 v[96:111], a[120:123], a[12:15], v[96:111]\n"
    "s_waitcnt vmcnt(5)\n"
    "s_barrier\n"
    "s_mov_b64 exec, s[10:11]\n"
    "buffer_store_dwordx2 v[62:63], v15, s[60:63], s28 offen sc1\n"
    "s_mov_b64 exec, -1\n"
    "s_add_u32 s73, s72, 0x20000\n"
    "s_nop 0\n"
    "v_mfma_f32_32x32x16_bf16 v[80:95], a[124:127], a[8:11], v[80:95]\n"
    "buffer_load_dwordx4 a[60:63], v247, s[60:63], s67 offen sc1\n"
    "v_readlane_b32 s67, v244, 9\n"
    "buffer_load_dwordx4 a[64:67], v246, s[60:63], s99 offen sc1\n"
    "v_lshl_add_u32 v133, v133, 13, v134\n"
    "v_add_u32 v5, 64, v133\n"
    "v_add_u32 v6, 48, v133\n"
    "v_mfma_f32_32x32x16_bf16 v[112:127], a[124:127], a[12:15], v[112:127]\n"
    "buffer_load_dwordx4 a[68:71], v247, s[60:63], s99 offen sc1\n"
    "s_lshl_b32 s16, s22, 16\n"
    "s_lshl_b32 s18, s23, 7\n"
    "s_add_u32 s75, s16, s18\n"
    "s_lshl_b32 s18, s12, 11\n"
    "v_mfma_f32_32x32x16_bf16 v[64:79], a[128:131], a[16:19], v[64:79]\n"
    "v_readlane_b32 s99, v244, 10\n"
    "buffer_load_dwordx4 a[72:75], v246, s[60:63], s67 offen sc1\n"
    "s_add_u32 s74, s75, s18\n"
    "s_mul_i32 s76, s12, 0x820\n"
    "s_and_b32 s16, s12, 1\n"
    "s_lshr_b32 s18, s12, 1\n"
    "v_mfma_f32_32x32x16_bf16 v[96:111], a[128:131], a[20:23], v[96:111]\n"
    "buffer_load_dwordx4 a[76:79], v247, s[60:63], s67 offen sc1\n"
    "v_lshl_add_u32 v133, v129, 2, v130\n"
    "v_mul_u32_u24 v133, 0x90, v133\n"
    "v_lshl_add_u32 v133, v132, 5, v133\n"
    "v_lshl_add_u32 v133, v131, 3, v133\n"
    "s_mul_i32 s19, s18, 0x1200\n"
    "v_mfma_f32_32x32x16_bf16 v[80:95], a[132:135], a[16:19], v[80:95]\n"
    "v_readlane_b32 s67, v244, 11\n"
    "buffer_load_dwordx4 a[80:83], v246, s[60:63], s99 offen sc1\n"
    "s_lshl_b32 s17, s16, 6\n"
    "s_add_u32 s19, s19, s17\n"
    "s_add_u32 s19, s19, 0x14b00\n"
    "v_add_u32 v4, s19, v133\n"
    "v_mfma_f32_32x32x16_bf16 v[112:127], a[132:135], a[20:23], v[112:127]\n"
    "buffer_load_dwordx4 a[84:87], v247, s[60:63], s99 offen sc1\n"
    "v_and_b32 v133, 7, v128\n"
    "v_mul_u32_u24 v133, 0x410, v133\n"
    "v_lshrrev_b32 v134, 3, v128\n"
    "v_lshl_add_u32 v133, v134, 7, v133\n"
    "v_mfma_f32_32x32x16_bf16 v[64:79], a[136:139], a[24:27], v[64:79]\n"
    "v_readlane_b32 s99, v244, 12\n"
    "buffer_load_dwordx4 a[88:91], v246, s[60:63], s67 offen sc1\n"
    "v_lshl_add_u32 v133, v129, 3, v133\n"
    "s_lshl_b32 s19, s16, 9\n"
    "s_lshl_b32 s17, s18, 6\n"
    "s_add_u32 s19, s19, s17\n"
    "v_mfma_f32_32x32x16_bf16 v[96:111], a[136:139], a[28:31], v[96:111]\n"
    "buffer_load_dwordx4 a[92:95], v247, s[60:63], s67 offen sc1\n"
    "s_add_u32 s19, s19, 0x16f00\n"
    "v_add_u32 v7, s19, v133\n"
    "s_lshl_b32 s19, s16, 15\n"
    "s_lshl_b32 s17, s18, 6\n"
    "s_add_u32 s19, s19, s17\n"
    "v_mfma_f32_32x32x16_bf16 v[80:95], a[140:143], a[24:27], v[80:95]\n"
    "v_readlane_b32 s67, v244, 13\n"
    "buffer_load_dwordx4 a[96:99], v246, s[60:63], s99 offen sc1\n"
    "v_lshlrev_b32 v133, 10, v128\n"
    "v_lshl_add_u32 v133, v129, 4, v133\n"
    "v_add_u32 v8, s19, v133\n"
    "v_add_u32 v9, s90, v13\n"
    "v_mfma_f32_32x32x16_bf16 v[112:127], a[140:143], a[28:31], v[112:127]\n"
    "buffer_load_dwordx4 a[100:103], v247, s[60:63], s99 offen sc1\n"
    "v_add_u32 v10, s91, v13\n"
    "s_xor_b32 s16, s12, 1\n"
    "s_mul_i32 s16, s16, 0x2a80\n"
    "v_add_u32 v11, s16, v13\n"
    "v_mfma_f32_32x32x16_bf16 v[64:79], a[144:147], a[32:35], v[64:79]\n"
    "v_readlane_b32 s99, v244, 14\n"
    "buffer_load_dwordx4 a[104:107], v246, s[60:63], s67 offen sc1\n"
    "s_xor_b32 s16, s12, 2\n"
    "s_mul_i32 s16, s16, 0x2a80\n"
    "s_add_u32 s16, s16, 0x1000\n"
    "v_add_u32 v20, s16, v13\n"
    "v_mfma_f32_32x32x16_bf16 v[96:111], a[144:147], a[36:39], v[96:111]\n"
    "buffer_load_dwordx4 a[108:111], v247, s[60:63], s67 offen sc1\n"
    "s_xor_b32 s16, s12, 3\n"
    "s_mul_i32 s16, s16, 0x2a80\n"
    "s_add_u32 s16, s16, 0x1d4c0\n"
    "v_add_u32 v21, s16, v13\n"
    "v_add_f32 v24, v24, v26\n"
    "v_mfma_f32_32x32x16_bf16 v[80:95], a[148:151], a[32:35], v[80:95]\n"
    "v_add_f32 v25, v25, v27\n"
    "v_add_f32 v28, v28, v30\n"
    "v_add_f32 v29, v29, v31\n"
    "v_add_f32 v24, v24, v28\n"
    "v_add_f32 v25, v25, v29\n"
    "v_mov_b32 v26, v24\n"
    "v_mfma_f32_32x32x16_bf16 v[112:127], a[148:151], a[36:39], v[112:127]\n"
    "v_mov_b32 v27, v25\n"
    "v_mov_b32 v37, 0x3727c5ac\n"
    "v_permlane32_swap_b32 v24, v26\n"
    "v_permlane32_swap_b32 v25, v27\n"
    "v_add_f32 v24, v24, v26\n"
    "s_waitcnt vmcnt(16)\n"
    "v_mfma_f32_32x32x16_bf16 v[64:79], a[152:155], a[40:43], v[64:79]\n"
    "v_readlane_b32 s67, v244, 15\n"
    "buffer_load_dwordx4 a[112:115], v246, s[60:63], s99 offen sc1\n"
    "buffer_load_dwordx4 a[116:119], v247, s[60:63], s99 offen sc1\n"
    "v_add_f32 v25, v25, v27\n"
    "v_mul_f32 v36, 0xbb000000, v24\n"
    "v_mul_f32 v25, 0x3b000000, v25\n"
    "v_mfma_f32_32x32x16_bf16 v[96:111], a[152:155], a[44:47], v[96:111]\n"
    "v_fma_f32 v25, -v36, v36, v25\n"
    "v_max_f32 v25, 0, v25\n"
    "v_add_f32 v154, v37, v25\n"
    "v_cvt_pk_bf16_f32 v36, v36, v36\n"
    "v_rsq_f32 v153, v154\n"
    "v_mfma_f32_32x32x16_bf16 v[80:95], a[156:159], a[40:43], v[80:95]\n"
    "v_and_b32 v36, 0xffff0000, v36\n"
    "v_mul_f32 v156, v36, v153\n"
    "v_mfma_f32_32x32x16_bf16 v[112:127], a[156:159], a[44:47], v[112:127]\n"
    "s_waitcnt vmcnt(16)\n"
    "v_mfma_f32_32x32x16_bf16 v[64:79], a[160:163], a[48:51], v[64:79]\n"
    "buffer_load_dwordx4 a[120:123], v246, s[60:63], s67 offen sc1\n"
    "v_readlane_b32 s99, v244, 16\n"
    "buffer_load_dwordx4 a[124:127], v247, s[60:63], s67 offen sc1\n"
    "s_add_u32 m0, s76, 0x16f00\n"
    "v_mfma_f32_32x32x16_bf16 v[96:111], a[160:163], a[52:55], v[96:111]\n"
    "v_readlane_b32 s67, v244, 17\n"
    "buffer_load_dwordx4 v5, s[36:39], s74 offen lds\n"
    "buffer_load_dwordx4 v6, s[36:39], s74 offen offset:1040 lds\n"
    "v_mfma_f32_32x32x16_bf16 v[80:95], a[164:167], a[48:51], v[80:95]\n"
    "v_mfma_f32_32x32x16_bf16 v[112:127], a[164:167], a[52:55], v[112:127]\n"
    "s_waitcnt vmcnt(18)\n"
    "v_mfma_f32_32x32x16_bf16 v[64:79], a[168:171], a[56:59], v[64:79]\n"
    "buffer_load_dwordx4 a[128:131], v246, s[60:63], s99 offen sc1\n"
    "buffer_load_dwordx4 a[132:135], v247, s[60:63], s99 offen sc1\n"
    "v_mfma_f32_32x32x16_bf16 v[96:111], a[168:171], a[60:63], v[96:111]\n"
    "v_mfma_f32_32x32x16_bf16 v[80:95], a[172:175], a[56:59], v[80:95]\n"
    "v_mfma_f32_32x32x16_bf16 v[112:127], a[172:175], a[60:63], v[112:127]\n"
    "s_waitcnt vmcnt(18)\n"
    "v_mfma_f32_32x32x16_bf16 v[64:79], a[176:179], a[64:67], v[64:79]\n"
    "v_readlane_b32 s99, v244, 18\n"
    "buffer_load_dwordx4 a[136:139], v246, s[60:63], s67 offen sc1\n"
    "buffer_load_dwordx4 a[140:143], v247, s[60:63], s67 offen sc1\n"
    "v_mfma_f32_32x32x16_bf16 v[96:111], a[176:179], a[68:71], v[96:111]\n"
    "v_mfma_f32_32x32x16_bf16 v[80:95], a[180:183], a[64:67], v[80:95]\n"
    "v_mfma_f32_32x32x16_bf16 v[112:127], a[180:183], a[68:71], v[112:127]\n"
    "s_waitcnt vmcnt(18)\n"
    "v_mfma_f32_32x32x16_bf16 v[64:79], a[184:187], a[72:75], v[64:79]\n"
    "v_readlane_b32 s67, v244, 19\n"
    "buffer_load_dwordx4 a[144:147], v246, s[60:63], s99 offen sc1\n"
    "buffer_load_dwordx4 a[148:151], v247, s[60:63], s99 offen sc1\n"
    "v_mfma_f32_32x32x16_bf16 v[96:111], a[184:187], a[76:79], v[96:111]\n"
    "v_mfma_f32_32x32x16_bf16 v[80:95], a[188:191], a[72:75], v[80:95]\n"
    "v_mfma_f32_32x32x16_bf16 v[112:127], a[188:191], a[76:79], v[112:127]\n"
    "s_waitcnt vmcnt(18)\n"
    "v_mfma_f32_32x32x16_bf16 v[64:79], a[192:195], a[80:83], v[64:79]\n"
    "v_readlane_b32 s99, v244, 20\n"
    "buffer_load_dwordx4 a[152:155], v246, s[60:63], s67 offen sc1\n"
    "buffer_load_dwordx4 a[156:159], v247, s[60:63], s67 offen sc1\n"
    "v_mfma_f32_32x32x16_bf16 v[96:111], a[192:195], a[84:87], v[96:111]\n"
    "v_mfma_f32_32x32x16_bf16 v[80:95], a[196:199], a[80:83], v[80:95]\n"
    "v_mfma_f32_32x32x16_bf16 v[112:127], a[196:199], a[84:87], v[112:127]\n"
    "s_waitcnt vmcnt(18)\n"
    "v_mfma_f32_32x32x16_bf16 v[64:79], a[200:203], a[88:91], v[64:79]\n"
    "v_readlane_b32 s67, v244, 21\n"
    "buffer_load_dwordx4 a[160:163], v246, s[60:63], s99 offen sc1\n"
    "buffer_load_dwordx4 a[164:167], v247, s[60:63], s99 offen sc1\n"
    "v_mfma_f32_32x32x16_bf16 v[96:111], a[200:203], a[92:95], v[96:111]\n"
    "v_mfma_f32_32x32x16_bf16 v[80:95], a[204:207], a[88:91], v[80:95]\n"
    "v_mfma_f32_32x32x16_bf16 v[112:127], a[204:207], a[92:95], v[112:127]\n"
    "s_waitcnt vmcnt(18)\n"
    "v_mfma_f32_32x32x16_bf16 v[64:79], a[208:211], a[96:99], v[64:79]\n"
    "ds_read_b64_tr_b16 v[208:209], v250\n"
    "ds_read_b64_tr_b16 v[210:211], v250 offset:512\n"
    "v_readlane_b32 s99, v244, 22\n"
    "buffer_load_dwordx4 a[168:171], v246, s[60:63], s67 offen sc1\n"
    "buffer_load_dwordx4 a[172:175], v247, s[60:63], s67 offen sc1\n"
    "v_mfma_f32_32x32x16_bf16 v[96:111], a[208:211], a[100:103], v[96:111]\n"
    "ds_read_b64_tr_b16 v[212:213], v251\n"
    "ds_read_b64_tr_b16 v[214:215], v251 offset:512\n"
    "v_mfma_f32_32x32x16_bf16 v[80:95], a[212:215], a[96:99], v[80:95]\n"
    "v_mfma_f32_32x32x16_bf16 v[112:127], a[212:215], a[100:103], v[112:127]\n"
    "s_waitcnt vmcnt(18)\n"
    "v_mfma_f32_32x32x16_bf16 v[64:79], a[216:219], a[104:107], v[64:79]\n"
    "ds_read_b64_tr_b16 v[192:193], v250 offset:2176\n"
    "ds_read_b64_tr_b16 v[194:195], v250 offset:2688\n"
    "v_readlane_b32 s67, v244, 23\n"
    "buffer_load_dwordx4 a[176:179], v246, s[60:63], s99 offen sc1\n"
    "buffer_load_dwordx4 a[180:183], v247, s[60:63], s99 offen sc1\n"
    "v_mfma_f32_32x32x16_bf16 v[96:111], a[216:219], a[108:111], v[96:111]\n"
    "ds_read_b64_tr_b16 v[196:197], v251 offset:2176\n"
    "ds_read_b64_tr_b16 v[198:199], v251 offset:2688\n"
    "v_mfma_f32_32x32x16_bf16 v[80:95], a[220:223], a[104:107], v[80:95]\n"
    "v_mfma_f32_32x32x16_bf16 v[112:127], a[220:223], a[108:111], v[112:127]\n"
    "s_waitcnt vmcnt(18) lgkmcnt(4)\n"
    "v_mfma_f32_32x32x16_bf16 v[64:79], v[208:211], a[112:115], v[64:79]\n"
    "ds_read_b64_tr_b16 v[200:201], v250 offset:4352\n"
    "ds_read_b64_tr_b16 v[202:203], v250 offset:4864\n"
    "v_readlane_b32 s99, v244, 24\n"
    "buffer_load_dwordx4 a[184:187], v246, s[60:63], s67 offen sc1\n"
    "buffer_load_dwordx4 a[188:191], v247, s[60:63], s67 offen sc1\n"
    "v_mfma_f32_32x32x16_bf16 v[96:111], v[208:211], a[116:119], v[96:111]\n"
    "ds_read_b64_tr_b16 v[204:205], v251 offset:4352\n"
    "ds_read_b64_tr_b16 v[206:207], v251 offset:4864\n"
    "v_readlane_b32 s67, v244, 25\n"
    "buffer_load_dwordx2 v[254:255], v22, s[60:63], s26 offen sc1\n"
    "s_waitcnt vmcnt(17)\n"
    "s_barrier\n"
    "v_mfma_f32_32x32x16_bf16 v[80:95], v[212:215], a[112:115], v[80:95]\n"
    "ds_read_b64 v[160:161], v7\n"
    "ds_read_b64 v[162:163], v7 offset:16\n"
    "ds_read_b64 v[164:165], v7 offset:32\n"
    "ds_read_b64 v[166:167], v7 offset:48\n"
    "s_waitcnt lgkmcnt(3)\n"
    "v_lshlrev_b32 v176, 16, v160\n"
    "v_fma_f32 v216, v176, v153, v156\n"
    "v_mfma_f32_32x32x16_bf16 v[112:127], v[212:215], a[116:119], v[112:127]\n"
    "v_and_b32 v177, 0xffff0000, v160\n"
    "v_fma_f32 v217, v177, v153, v156\n"
    "v_lshlrev_b32 v178, 16, v161\n"
    "v_fma_f32 v218, v178, v153, v156\n"
    "v_and_b32 v179, 0xffff0000, v161\n"
    "v_mfma_f32_32x32x16_bf16 v[64:79], v[192:195], a[120:123], v[64:79]\n"
    "ds_read_b64_tr_b16 v[208:209], v250 offset:6528\n"
    "ds_read_b64_tr_b16 v[210:211], v250 offset:7040\n"
    "buffer_load_dwordx4 a[192:195], v246, s[60:63], s99 offen sc1\n"
    "buffer_load_dwordx4 a[196:199], v247, s[60:63], s99 offen sc1\n"
    "v_fma_f32 v219, v179, v153, v156\n"
    "v_mfma_f32_32x32x16_bf16 v[96:111], v[192:195], a[124:127], v[96:111]\n"
    "ds_read_b64_tr_b16 v[212:213], v251 offset:6528\n"
    "ds_read_b64_tr_b16 v[214:215], v251 offset:7040\n"
    "s_waitcnt lgkmcnt(6)\n"
    "v_lshlrev_b32 v180, 16, v162\n"
    "v_fma_f32 v220, v180, v153, v156\n"
    "v_and_b32 v181, 0xffff0000, v162\n"
    "v_fma_f32 v221, v181, v153, v156\n"
    "v_mfma_f32_32x32x16_bf16 v[80:95], v[196:199], a[120:123], v[80:95]\n"
    "v_lshlrev_b32 v182, 16, v163\n"
    "v_fma_f32 v222, v182, v153, v156\n"
    "v_and_b32 v183, 0xffff0000, v163\n"
    "v_fma_f32 v223, v183, v153, v156\n"
    "s_waitcnt lgkmcnt(5)\n"
    "v_lshlrev_b32 v184, 16, v164\n"
    "v_fma_f32 v224, v184, v153, v156\n"
    "v_mfma_f32_32x32x16_bf16 v[112:127], v[196:199], a[124:127], v[112:127]\n"
    "v_and_b32 v185, 0xffff0000, v164\n"
    "v_fma_f32 v225, v185, v153, v156\n"
    "v_lshlrev_b32 v186, 16, v165\n"
    "v_fma_f32 v226, v186, v153, v156\n"
    "v_and_b32 v187, 0xffff0000, v165\n"
    "s_waitcnt vmcnt(17)\n"
    "v_mfma_f32_32x32x16_bf16 v[64:79], v[200:203], a[128:131], v[64:79]\n"
    "ds_read_b64_tr_b16 v[192:193], v250 offset:8704\n"
    "ds_read_b64_tr_b16 v[194:195], v250 offset:9216\n"
    "v_readlane_b32 s99, v244, 26\n"
    "buffer_load_dwordx4 a[200:203], v246, s[60:63], s67 offen sc1\n"
    "buffer_load_dwordx4 a[204:207], v247, s[60:63], s67 offen sc1\n"
    "v_mfma_f32_32x32x16_bf16 v[96:111], v[200:203], a[132:135], v[96:111]\n"
    "ds_read_b64_tr_b16 v[196:197], v251 offset:8704\n"
    "ds_read_b64_tr_b16 v[198:199], v251 offset:9216\n"
    "v_fma_f32 v227, v187, v153, v156\n"
    "s_waitcnt lgkmcnt(8)\n"
    "v_lshlrev_b32 v188, 16, v166\n"
    "v_fma_f32 v228, v188, v153, v156\n"
    "v_and_b32 v189, 0xffff0000, v166\n"
    "v_mfma_f32_32x32x16_bf16 v[80:95], v[204:207], a[128:131], v[80:95]\n"
    "v_fma_f32 v229, v189, v153, v156\n"
    "v_lshlrev_b32 v190, 16, v167\n"
    "v_fma_f32 v230, v190, v153, v156\n"
    "v_and_b32 v191, 0xffff0000, v167\n"
    "v_fma_f32 v231, v191, v153, v156\n"
    "v_mfma_f32_32x32x16_bf16 v[112:127], v[204:207], a[132:135], v[112:127]\n"
    "s_waitcnt vmcnt(17) lgkmcnt(4)\n"
    "v_mfma_f32_32x32x16_bf16 v[64:79], v[208:211], a[136:139], v[64:79]\n"
    "ds_read_b64_tr_b16 v[200:201], v248\n"
    "ds_read_b64_tr_b16 v[202:203], v248 offset:512\n"
    "v_readlane_b32 s67, v244, 27\n"
    "buffer_load_dwordx4 a[208:211], v246, s[60:63], s99 offen sc1\n"
    "buffer_load_dwordx4 a[212:215], v247, s[60:63], s99 offen sc1\n"
    "v_mfma_f32_32x32x16_bf16 v[96:111], v[208:211], a[140:143], v[96:111]\n"
    "ds_read_b64_tr_b16 v[204:205], v249\n"
    "ds_read_b64_tr_b16 v[206:207], v249 offset:512\n"
    "v_mfma_f32_32x32x16_bf16 v[80:95], v[212:215], a[136:139], v[80:95]\n"
    "v_mfma_f32_32x32x16_bf16 v[112:127], v[212:215], a[140:143], v[112:127]\n"
    "s_waitcnt vmcnt(17) lgkmcnt(4)\n"
    "v_mfma_f32_32x32x16_bf16 v[64:79], v[192:195], a[144:147], v[64:79]\n"
    "ds_read_b64_tr_b16 v[208:209], v248 offset:2176\n"
    "ds_read_b64_tr_b16 v[210:211], v248 offset:2688\n"
    "buffer_load_dwordx4 a[216:219], v246, s[60:63], s67 offen sc1\n"
    "buffer_load_dwordx4 a[220:223], v247, s[60:63], s67 offen sc1\n"
    "v_mfma_f32_32x32x16_bf16 v[96:111], v[192:195], a[148:151], v[96:111]\n"
    "ds_read_b64_tr_b16 v[212:213], v249 offset:2176\n"
    "ds_read_b64_tr_b16 v[214:215], v249 offset:2688\n"
    "v_mfma_f32_32x32x16_bf16 v[80:95], v[196:199], a[144:147], v[80:95]\n"
    "v_mfma_f32_32x32x16_bf16 v[112:127], v[196:199], a[148:151], v[112:127]\n"
    "s_waitcnt vmcnt(17) lgkmcnt(4)\n"
    "v_mfma_f32_32x32x16_bf16 v[64:79], v[200:203], a[152:155], v[64:79]\n"
    "ds_read_b64_tr_b16 v[192:193], v248 offset:4352\n"
    "ds_read_b64_tr_b16 v[194:195], v248 offset:4864\n"
    "s_waitcnt vmcnt(8)\n"
    "v_cmp_ne_u64 vcc, s[8:9], v[254:255]\n"
    "s_nop 1\n"
    "s_cbranch_vccnz .Lspin4_%=\n"
    ".Lret4_%=:\n"
    "v_mfma_f32_32x32x16_bf16 v[96:111], v[200:203], a[156:159], v[96:111]\n"
    "ds_read_b64_tr_b16 v[196:197], v249 offset:4352\n"
    "ds_read_b64_tr_b16 v[198:199], v249 offset:4864\n"
    "buffer_load_dwordx4 v[144:147], v2, s[60:63], s72 offen sc1\n"
    "buffer_load_dwordx4 v[148:151], v2, s[60:63], s73 offen sc1\n"
    "v_mfma_f32_32x32x16_bf16 v[80:95], v[204:207], a[152:155], v[80:95]\n"
    "v_mfma_f32_32x32x16_bf16 v[112:127], v[204:207], a[156:159], v[112:127]\n"
    "s_waitcnt lgkmcnt(4)\n"
    "v_mfma_f32_32x32x16_bf16 v[64:79], v[208:211], a[160:163], v[64:79]\n"
    "ds_read_b64_tr_b16 v[200:201], v248 offset:6528\n"
    "ds_read_b64_tr_b16 v[202:203], v248 offset:7040\n"
    "v_mfma_f32_32x32x16_bf16 v[96:111], v[208:211], a[164:167], v[96:111]\n"
    "ds_read_b64_tr_b16 v[204:205], v249 offset:6528\n"
    "ds_read_b64_tr_b16 v[206:207], v249 offset:7040\n"
    "v_mfma_f32_32x32x16_bf16 v[80:95], v[212:215], a[160:163], v[80:95]\n"
    "v_mfma_f32_32x32x16_bf16 v[112:127], v[212:215], a[164:167], v[112:127]\n"
    "s_waitcnt lgkmcnt(4)\n"
    "v_mfma_f32_32x32x16_bf16 v[64:79], v[192:195], a[168:171], v[64:79]\n"
    "ds_read_b64_tr_b16 v[208:209], v248 offset:8704\n"
    "ds_read_b64_tr_b16 v[210:211], v248 offset:9216\n"
    "v_mfma_f32_32x32x16_bf16 v[96:111], v[192:195], a[172:175], v[96:111]\n"
    "ds_read_b64_tr_b16 v[212:213], v249 offset:8704\n"
    "ds_read_b64_tr_b16 v[214:215], v249 offset:9216\n"
    "v_mfma_f32_32x32x16_bf16 v[80:95], v[196:199], a[168:171], v[80:95]\n"
    "v_mfma_f32_32x32x16_bf16 v[112:127], v[196:199], a[172:175], v[112:127]\n"
    "s_waitcnt lgkmcnt(4)\n"
    "v_mfma_f32_32x32x16_bf16 v[64:79], v[200:203], a[176:179], v[64:79]\n"
    "ds_read_b64_tr_b16 v[192:193], v252\n"
    "ds_read_b64_tr_b16 v[194:195], v252 offset:512\n"
    "v_mfma_f32_32x32x16_bf16 v[96:111], v[200:203], a[180:183], v[96:111]\n"
    "ds_read_b64_tr_b16 v[196:197], v253\n"
    "ds_read_b64_tr_b16 v[198:199], v253 offset:512\n"
    "v_mfma_f32_32x32x16_bf16 v[80:95], v[204:207], a[176:179], v[80:95]\n"
    "v_mfma_f32_32x32x16_bf16 v[112:127], v[204:207], a[180:183], v[112:127]\n"
    "s_waitcnt lgkmcnt(4)\n"
    "v_mfma_f32_32x32x16_bf16 v[64:79], v[208:211], a[184:187], v[64:79]\n"
    "ds_read_b64_tr_b16 v[200:201], v252 offset:2176\n"
    "ds_read_b64_tr_b16 v[202:203], v252 offset:2688\n"
    "v_mfma_f32_32x32x16_bf16 v[96:111], v[208:211], a[188:191], v[96:111]\n"
    "ds_read_b64_tr_b16 v[204:205], v253 offset:2176\n"
    "ds_read_b64_tr_b16 v[206:207], v253 offset:2688\n"
    "v_mfma_f32_32x32x16_bf16 v[80:95], v[212:215], a[184:187], v[80:95]\n"
    "v_mfma_f32_32x32x16_bf16 v[112:127], v[212:215], a[188:191], v[112:127]\n"
    "s_waitcnt vmcnt(8) lgkmcnt(4)\n"
    "v_mfma_f32_32x32x16_bf16 v[64:79], v[192:195], a[192:195], v[64:79]\n"
    "ds_read_b64_tr_b16 v[208:209], v252 offset:4352\n"
    "ds_read_b64_tr_b16 v[210:211], v252 offset:4864\n"
    "v_mfma_f32_32x32x16_bf16 v[96:111], v[192:195], a[196:199], v[96:111]\n"
    "ds_read_b64_tr_b16 v[212:213], v253 offset:4352\n"
    "ds_read_b64_tr_b16 v[214:215], v253 offset:4864\n"
    "v_mfma_f32_32x32x16_bf16 v[80:95], v[196:199], a[192:195], v[80:95]\n"
    "v_mfma_f32_32x32x16_bf16 v[112:127], v[196:199], a[196:199], v[112:127]\n"
    "s_waitcnt vmcnt(6) lgkmcnt(4)\n"
    "v_mfma_f32_32x32x16_bf16 v[64:79], v[200:203], a[200:203], v[64:79]\n"
    "ds_read_b64_tr_b16 v[192:193], v252 offset:6528\n"
    "ds_read_b64_tr_b16 v[194:195], v252 offset:7040\n"
    "v_mfma_f32_32x32x16_bf16 v[96:111], v[200:203], a[204:207], v[96:111]\n"
    "ds_read_b64_tr_b16 v[196:197], v253 offset:6528\n"
    "ds_read_b64_tr_b16 v[198:199], v253 offset:7040\n"
    "v_mfma_f32_32x32x16_bf16 v[80:95], v[204:207], a[200:203], v[80:95]\n"
    "v_mfma_f32_32x32x16_bf16 v[112:127], v[204:207], a[204:207], v[112:127]\n"
    "s_waitcnt vmcnt(4) lgkmcnt(4)\n"
    "v_mfma_f32_32x32x16_bf16 v[64:79], v[208:211], a[208:211], v[64:79]\n"
    "v_mfma_f32_32x32x16_bf16 v[96:111], v[208:211], a[212:215], v[96:111]\n"
    "v_mfma_f32_32x32x16_bf16 v[80:95], v[212:215], a[208:211], v[80:95]\n"
    "v_mfma_f32_32x32x16_bf16 v[112:127], v[212:215], a[212:215], v[112:127]\n"
    "s_waitcnt vmcnt(2) lgkmcnt(0)\n"
    "v_mfma_f32_32x32x16_bf16 v[64:79], v[192:195], a[216:219], v[64:79]\n"
    "v_mfma_f32_32x32x16_bf16 v[96:111], v[192:195], a[220:223], v[96:111]\n"
    "v_mfma_f32_32x32x16_bf16 v[80:95], v[196:199], a[216:219], v[80:95]\n"
    "v_mfma_f32_32x32x16_bf16 v[112:127], v[196:199], a[220:223], v[112:127]\n"
    "s_waitcnt vmcnt(1)\n"
    "ds_write_b128 v3, v[144:147]\n"
    "s_waitcnt vmcnt(0)\n"
    "ds_write_b128 v3, v[148:151] offset:4608\n"
    "s_nop 5\n"
    "ds_write_b128 v9, v[96:99]\n"
    "ds_write_b128 v9, v[100:103] offset:1024\n"
    "ds_write_b128 v9, v[104:107] offset:2048\n"
    "ds_write_b128 v9, v[108:111] offset:3072\n"
    "ds_write_b128 v9, v[80:83] offset:4096\n"
    "ds_write_b128 v9, v[84:87] offset:5120\n"
    "ds_write_b128 v9, v[88:91] offset:6144\n"
    "ds_write_b128 v9, v[92:95] offset:7168\n"
    "ds_write_b128 v10, v[112:115]\n"
    "ds_write_b128 v10, v[116:119] offset:1024\n"
    "ds_write_b128 v10, v[120:123] offset:2048\n"
    "ds_write_b128 v10, v[124:127] offset:3072\n"
    "v_pk_add_f32 v[64:65], v[64:65], v[176:177]\n"
    "v_pk_add_f32 v[66:67], v[66:67], v[178:179]\n"
    "v_pk_add_f32 v[68:69], v[68:69], v[180:181]\n"
    "v_pk_add_f32 v[70:71], v[70:71], v[182:183]\n"
    "v_pk_add_f32 v[72:73], v[72:73], v[184:185]\n"
    "v_pk_add_f32 v[74:75], v[74:75], v[186:187]\n"
    "v_pk_add_f32 v[76:77], v[76:77], v[188:189]\n"
    "v_pk_add_f32 v[78:79], v[78:79], v[190:191]\n"
    "s_waitcnt lgkmcnt(0)\n"
    "s_barrier\n"
    "ds_read_b64_tr_b16 v[168:169], v4\n"
    "ds_read_b64_tr_b16 v[170:171], v4 offset:1152\n"
    "ds_read_b64_tr_b16 v[172:173], v4 offset:2304\n"
    "ds_read_b64_tr_b16 v[174:175], v4 offset:3456\n"
    "ds_read_b128 v[80:83], v11\n"
    "ds_read_b128 v[96:99], v20\n"
    "ds_read_b128 v[112:115], v21\n"
    "ds_read_b128 v[84:87], v11 offset:1024\n"
    "ds_read_b128 v[100:103], v20 offset:1024\n"
    "ds_read_b128 v[116:119], v21 offset:1024\n"
    "ds_read_b128 v[88:91], v11 offset:2048\n"
    "ds_read_b128 v[104:107], v20 offset:2048\n"
    "ds_read_b128 v[120:123], v21 offset:2048\n"
    "ds_read_b128 v[92:95], v11 offset:3072\n"
    "ds_read_b128 v[108:111], v20 offset:3072\n"
    "ds_read_b128 v[124:127], v21 offset:3072\n"
    "s_waitcnt lgkmcnt(15)\n"
    "v_lshlrev_b32 v200, 16, v168\n"
    "v_and_b32 v201, 0xffff0000, v168\n"
    "v_lshlrev_b32 v202, 16, v169\n"
    "v_and_b32 v203, 0xffff0000, v169\n"
    "v_pk_fma_f32 v[64:65], v[216:217], v[200:201], v[64:65]\n"
    "v_pk_fma_f32 v[66:67], v[218:219], v[202:203], v[66:67]\n"
    "s_waitcnt lgkmcnt(10)\n"
    "v_pk_add_f32 v[80:81], v[80:81], v[96:97]\n"
    "v_pk_add_f32 v[82:83], v[82:83], v[98:99]\n"
    "s_waitcnt lgkmcnt(9)\n"
    "v_pk_add_f32 v[64:65], v[64:65], v[112:113]\n"
    "v_pk_add_f32 v[66:67], v[66:67], v[114:115]\n"
    "v_pk_add_f32 v[64:65], v[64:65], v[80:81]\n"
    "v_pk_add_f32 v[66:67], v[66:67], v[82:83]\n"
    "v_cvt_pk_bf16_f32 v192, v64, v65\n"
    "v_cvt_pk_bf16_f32 v193, v66, v67\n"
    "v_lshlrev_b32 v200, 16, v170\n"
    "v_and_b32 v201, 0xffff0000, v170\n"
    "v_lshlrev_b32 v202, 16, v171\n"
    "v_and_b32 v203, 0xffff0000, v171\n"
    "v_pk_fma_f32 v[68:69], v[220:221], v[200:201], v[68:69]\n"
    "v_pk_fma_f32 v[70:71], v[222:223], v[202:203], v[70:71]\n"
    "s_waitcnt lgkmcnt(7)\n"
    "v_pk_add_f32 v[84:85], v[84:85], v[100:101]\n"
    "v_pk_add_f32 v[86:87], v[86:87], v[102:103]\n"
    "s_waitcnt lgkmcnt(6)\n"
    "v_pk_add_f32 v[68:69], v[68:69], v[116:117]\n"
    "v_pk_add_f32 v[70:71], v[70:71], v[118:119]\n"
    "v_pk_add_f32 v[68:69], v[68:69], v[84:85]\n"
    "v_pk_add_f32 v[70:71], v[70:71], v[86:87]\n"
    "v_cvt_pk_bf16_f32 v194, v68, v69\n"
    "v_cvt_pk_bf16_f32 v195, v70, v71\n"
    "s_nop 0\n"
    "v_permlane32_swap_b32 v192, v194\n"
    "v_permlane32_swap_b32 v193, v195\n"
    "buffer_store_dwordx4 v[192:195], v8, s[56:59], s75 offen\n"
    "v_lshlrev_b32 v200, 16, v172\n"
    "v_and_b32 v201, 0xffff0000, v172\n"
    "v_lshlrev_b32 v202, 16, v173\n"
    "v_and_b32 v203, 0xffff0000, v173\n"
    "v_pk_fma_f32 v[72:73], v[224:225], v[200:201], v[72:73]\n"
    "v_pk_fma_f32 v[74:75], v[226:227], v[202:203], v[74:75]\n"
    "s_waitcnt lgkmcnt(4)\n"
    "v_pk_add_f32 v[88:89], v[88:89], v[104:105]\n"
    "v_pk_add_f32 v[90:91], v[90:91], v[106:107]\n"
    "s_waitcnt lgkmcnt(3)\n"
    "v_pk_add_f32 v[72:73], v[72:73], v[120:121]\n"
    "v_pk_add_f32 v[74:75], v[74:75], v[122:123]\n"
    "v_pk_add_f32 v[72:73], v[72:73], v[88:89]\n"
    "v_pk_add_f32 v[74:75], v[74:75], v[90:91]\n"
    "v_cvt_pk_bf16_f32 v196, v72, v73\n"
    "v_cvt_pk_bf16_f32 v197, v74, v75\n"
    "v_lshlrev_b32 v200, 16, v174\n"
    "v_and_b32 v201, 0xffff0000, v174\n"
    "v_lshlrev_b32 v202, 16, v175\n"
    "v_and_b32 v203, 0xffff0000, v175\n"
    "v_pk_fma_f32 v[76:77], v[228:229], v[200:201], v[76:77]\n"
    "v_pk_fma_f32 v[78:79], v[230:231], v[202:203], v[78:79]\n"
    "s_waitcnt lgkmcnt(1)\n"
    "v_pk_add_f32 v[92:93], v[92:93], v[108:109]\n"
    "v_pk_add_f32 v[94:95], v[94:95], v[110:111]\n"
    "s_waitcnt lgkmcnt(0)\n"
    "v_pk_add_f32 v[76:77], v[76:77], v[124:125]\n"
    "v_pk_add_f32 v[78:79], v[78:79], v[126:127]\n"
    "v_pk_add_f32 v[76:77], v[76:77], v[92:93]\n"
    "v_pk_add_f32 v[78:79], v[78:79], v[94:95]\n"
    "v_cvt_pk_bf16_f32 v198, v76, v77\n"
    "v_cvt_pk_bf16_f32 v199, v78, v79\n"
    "s_nop 0\n"
    "v_permlane32_swap_b32 v196, v198\n"
    "v_permlane32_swap_b32 v197, v199\n"
    "buffer_store_dwordx4 v[196:199], v8, s[56:59], s75 offen offset:32\n"
    "s_branch .Lend_%=\n"
    ".Lspin0_%=:\n"
    "s_mov_b32 s33, 0\n"
    ".Lspl0_%=:\n"
    "s_sleep 1\n"
    "buffer_load_dwordx2 v[254:255], v52, s[60:63], 0 offen sc1\n"
    "s_waitcnt vmcnt(0)\n"
    "v_cmp_ne_u64 vcc, s[8:9], v[254:255]\n"
    "s_nop 1\n"
    "s_cbranch_vccz .Lret0_%=\n"
    "s_add_u32 s33, s33, 1\n"
    "s_cmp_lt_u32 s33, 0x40000\n"
    "s_cbranch_scc1 .Lspl0_%=\n"
    "s_branch .Lret0_%=\n"
    ".Lspin1_%=:\n"
    "s_mov_b32 s33, 0\n"
    ".Lspl1_%=:\n"
    "s_sleep 1\n"
    "buffer_load_dwordx2 v[254:255], v17, s[60:63], s24 offen sc1\n"
    "s_waitcnt vmcnt(0)\n"
    "v_cmp_ne_u64 vcc, s[8:9], v[254:255]\n"
    "s_nop 1\n"
    "s_cbranch_vccz .Lret1_%=\n"
    "s_add_u32 s33, s33, 1\n"
    "s_cmp_lt_u32 s33, 0x40000\n"
    "s_cbranch_scc1 .Lspl1_%=\n"
    "s_branch .Lret1_%=\n"
    ".Lspin2_%=:\n"
    "s_mov_b32 s33, 0\n"
    ".Lspl2_%=:\n"
    "s_sleep 1\n"
    "buffer_load_dwordx2 v[254:255], v17, s[60:63], s24 offen sc1\n"
    "s_waitcnt vmcnt(0)\n"
    "v_cmp_ne_u64 vcc, s[8:9], v[254:255]\n"
    "s_nop 1\n"
    "s_cbranch_vccz .Lret2_%=\n"
    "s_add_u32 s33, s33, 1\n"
    "s_cmp_lt_u32 s33, 0x40000\n"
    "s_cbranch_scc1 .Lspl2_%=\n"
    "s_branch .Lret2_%=\n"
    ".Lspin3_%=:\n"
    "s_mov_b32 s33, 0\n"
    ".Lspl3_%=:\n"
    "s_sleep 1\n"
    "buffer_load_dwordx2 v[254:255], v18, s[60:63], s25 offen sc1\n"
    "s_waitcnt vmcnt(0)\n"
    "v_cmp_ne_u64 vcc, s[8:9], v[254:255]\n"
    "s_nop 1\n"
    "s_cbranch_vccz .Lret3_%=\n"
    "s_add_u32 s33, s33, 1\n"
    "s_cmp_lt_u32 s33, 0x40000\n"
    "s_cbranch_scc1 .Lspl3_%=\n"
    "s_branch .Lret3_%=\n"
    ".Lspin4_%=:\n"
    "s_mov_b32 s33, 0\n"
    ".Lspl4_%=:\n"
    "s_sleep 1\n"
    "buffer_load_dwordx2 v[254:255], v22, s[60:63], s26 offen sc1\n"
    "s_waitcnt vmcnt(0)\n"
    "v_cmp_ne_u64 vcc, s[8:9], v[254:255]\n"
    "s_nop 1\n"
    "s_cbranch_vccz .Lret4_%=\n"
    "s_add_u32 s33, s33, 1\n"
    "s_cmp_lt_u32 s33, 0x40000\n"
    "s_cbranch_scc1 .Lspl4_%=\n"
    "s_branch .Lret4_%=\n"
    ".Lend_%=:\n"
    :
    : [ka] "s"(__builtin_amdgcn_kernarg_segment_ptr()), [tag] "s"(tag), [bid] "s"(b), [tid] "v"(threadIdx.x)
    : "v1","v2","v3","v4","v5","v6","v7","v8","v9","v10","v11","v12","v13","v14","v15","v16","v17","v18","v19","v20","v21","v22","v23","v24","v25","v26","v27","v28","v29","v30","v31","v32","v33","v34","v35","v36","v37","v38","v39","v40","v41","v42","v43","v44","v45","v46","v47","v48","v49","v50","v51","v52","v53","v54","v55","v56","v57","v58","v59","v60","v61","v62","v63","v64","v65","v66","v67","v68","v69","v70","v71","v72","v73","v74","v75","v76","v77","v78","v79","v80","v81","v82","v83","v84","v85","v86","v87","v88","v89","v90","v91","v92","v93","v94","v95","v96","v97","v98","v99","v100","v101","v102","v103","v104","v105","v106","v107","v108","v109","v110","v111","v112","v113","v114","v115","v116","v117","v118","v119","v120","v121","v122","v123","v124","v125","v126","v127","v128","v129","v130","v131","v132","v133","v134","v135","v136","v137","v138","v139","v140","v141","v142","v143","v144","v145","v146","v147","v148","v149","v150","v151","v152","v153","v154","v155","v156","v157","v158","v159","v160","v161","v162","v163","v164","v165","v166","v167","v168","v169","v170","v171","v172","v173","v174","v175","v176","v177","v178","v179","v180","v181","v182","v183","v184","v185","v186","v187","v188","v189","v190","v191","v192","v193","v194","v195","v196","v197","v198","v199","v200","v201","v202","v203","v204","v205","v206","v207","v208","v209","v210","v211","v212","v213","v214","v215","v216","v217","v218","v219","v220","v221","v222","v223","v224","v225","v226","v227","v228","v229","v230","v231","v232","v233","v234","v235","v236","v237","v238","v239","v240","v241","v242","v243","v244","v245","v246","v247","v248","v249","v250","v251","v252","v253","v254","v255","a0","a1","a2","a3","a4","a5","a6","a7","a8","a9","a10","a11","a12","a13","a14","a15","a16","a17","a18","a19","a20","a21","a22","a23","a24","a25","a26","a27","a28","a29","a30","a31","a32","a33","a34","a35","a36","a37","a38","a39","a40","a41","a42","a43","a44","a45","a46","a47","a48","a49","a50","a51","a52","a53","a54","a55","a56","a57","a58","a59","a60","a61","a62","a63","a64","a65","a66","a67","a68","a69","a70","a71","a72","a73","a74","a75","a76","a77","a78","a79","a80","a81","a82","a83","a84","a85","a86","a87","a88","a89","a90","a91","a92","a93","a94","a95","a96","a97","a98","a99","a100","a101","a102","a103","a104","a105","a106","a107","a108","a109","a110","a111","a112","a113","a114","a115","a116","a117","a118","a119","a120","a121","a122","a123","a124","a125","a126","a127","a128","a129","a130","a131","a132","a133","a134","a135","a136","a137","a138","a139","a140","a141","a142","a143","a144","a145","a146","a147","a148","a149","a150","a151","a152","a153","a154","a155","a156","a157","a158","a159","a160","a161","a162","a163","a164","a165","a166","a167","a168","a169","a170","a171","a172","a173","a174","a175","a176","a177","a178","a179","a180","a181","a182","a183","a184","a185","a186","a187","a188","a189","a190","a191","a192","a193","a194","a195","a196","a197","a198","a199","a200","a201","a202","a203","a204","a205","a206","a207","a208","a209","a210","a211","a212","a213","a214","a215","a216","a217","a218","a219","a220","a221","a222","a223","a224","a225","a226","a227","a228","a229","a230","a231","a232","a233","a234","a235","a236","a237","a238","a239","a240","a241","a242","a243","a244","a245","a246","a247","a248","a249","a250","a251","a252","a253","a254","a255","s8","s9","s10","s11","s12","s13","s14","s15","s16","s17","s18","s19","s20","s21","s22","s23","s24","s25","s26","s27","s28","s29","s30","s31","s33","s34","s35","s36","s37","s38","s39","s40","s41","s42","s43","s44","s45","s46","s47","s48","s49","s50","s51","s52","s53","s54","s55","s56","s57","s58","s59","s60","s61","s62","s63","s64","s65","s66","s67","s68","s69","s70","s71","s72","s73","s74","s75","s76","s77","s78","s79","s80","s81","s82","s83","s84","s85","s86","s87","s88","s89","s90","s91","s92","s93","s94","s95","s96","s97","s98","s99","vcc","scc","memory");
}
}

extern "C" void kernel_launch(void* const* d_in, const int* in_sizes, int n_in, void* d_out, int out_size, void* d_ws, size_t ws_size,
                              hipStream_t stream) {
  using namespace mk;
  (void)hipFuncSetAttribute((const void*)fused, hipFuncAttributeMaxDynamicSharedMemorySize, LDS_BYTES);
  fused<<<256, 256, LDS_BYTES, stream>>>((const u16*)d_in[0], (const u16*)d_in[4], (const u16*)d_in[5], (const u16*)d_in[7], (const u16*)d_in[8],
                                         (const u16*)d_in[9], (u16*)d_out, (char*)d_ws, (const u16*)d_in[1], (const u16*)d_in[2], (const u16*)d_in[3]);
}
```
